```python
import torch

def _softmax(x, axis=-1):
    return torch.softmax(x, dim=axis)

def _gelu(x):
    return torch.nn.functional.gelu(x, approximate='tanh')

def _silu(x):
    return torch.nn.functional.silu(x)

def _sigmoid(x):
    return torch.sigmoid(x)

def _layernorm(x, axis=-1, eps=1e-05):
    m = x.mean(dim=axis, keepdim=True)
    v = x.var(dim=axis, keepdim=True, unbiased=False)
    return (x - m) / torch.sqrt(v + eps)

def _scatter_add(tgt, idx, src, axis=0):
    return tgt.clone().index_add_(axis, idx, src)

def _segment_sum(src, seg, n):
    out = torch.zeros((n,) + src.shape[1:], dtype=src.dtype)
    return out.index_add_(0, seg, src)

def _pad(x, pw):
    flat = tuple((v for pair in reversed(pw) for v in pair))
    return torch.nn.functional.pad(x, flat)
kernel_name = 'standalone__video_video_swin__vision'

def setup_inputs():

    _w = lambda a: (lambda t: t.to(torch.bfloat16) if t.is_floating_point() else t)(torch.as_tensor(a))
    T = 4
    H = W = 224 // 7 * 7
    C, win = (96, 7)
    return {'x': _w((lambda *s: torch.randn(*s, dtype=torch.bfloat16) * 0.1)(1, T, H, W, C)), 'wqkv_t': _w((lambda *s: torch.randn(*s, dtype=torch.bfloat16) * 0.1)(C, 3 * C)), 'wo_t': _w((lambda *s: torch.randn(*s, dtype=torch.bfloat16) * 0.1)(C, C)), 'wqkv_s': _w((lambda *s: torch.randn(*s, dtype=torch.bfloat16) * 0.1)(C, 3 * C)), 'wo_s': _w((lambda *s: torch.randn(*s, dtype=torch.bfloat16) * 0.05)(C, C))}

def reference(x, wqkv_t, wo_t, wqkv_s, wo_s):
    B, T, H, W, C = x.shape
    win = 7
    scale = C ** (-0.5)
    h = _layernorm(x, axis=-1)
    ht = (lambda x, *d: x.permute(*d))(h, 0, 2, 3, 1, 4).reshape(B * H * W, T, C)
    qkv = ht @ wqkv_t
    q, k, v = (qkv[..., :C].clone(), qkv[..., C:2 * C].clone(), qkv[..., 2 * C:].clone())
    p = _softmax(torch.einsum('btc,bsc->bts', q, k) * scale, axis=-1)
    ot = torch.einsum('bts,bsc->btc', p, v) @ wo_t
    ot = (lambda x, *d: x.permute(*d))(ot.reshape(B, H, W, T, C), 0, 3, 1, 2, 4)
    x = x + ot
    h = _layernorm(x, axis=-1)
    nh, nw = (H // win, W // win)
    hs = h.reshape(B * T, nh, win, nw, win, C)
    hw = (lambda x, *d: x.permute(*d))(hs, 0, 1, 3, 2, 4, 5).reshape(B * T * nh * nw, win * win, C)
    qkv = hw @ wqkv_s
    q, k, v = (qkv[..., :C].clone(), qkv[..., C:2 * C].clone(), qkv[..., 2 * C:].clone())
    p = _softmax(torch.einsum('bqc,bkc->bqk', q, k) * scale, axis=-1)
    os = torch.einsum('bqk,bkc->bqc', p, v) @ wo_s
    os = (lambda x, *d: x.permute(*d))(os.reshape(B * T, nh, nw, win, win, C), 0, 1, 3, 2, 4, 5)
    os = os.reshape(B, T, H, W, C)
    return x + os
```

```cpp
#include <hip/hip_runtime.h>
#include <hip/hip_bf16.h>
#include <stdint.h>

#if defined(__HIP_DEVICE_COMPILE__)
#define NOPK __attribute__((target("no-packed-fp32-ops")))
#else
#define NOPK
#endif
#define DEV __device__ __forceinline__ NOPK
#define LAS __attribute__((address_space(3)))

typedef __attribute__((ext_vector_type(8))) __bf16 bf16x8;
typedef __attribute__((ext_vector_type(2))) __bf16 bf16x2;
typedef __attribute__((ext_vector_type(4))) short s16x4;
typedef __attribute__((ext_vector_type(16))) float f32x16;
typedef __attribute__((ext_vector_type(4))) float f32x4;
typedef __attribute__((ext_vector_type(2))) float f32x2;
typedef __attribute__((ext_vector_type(4))) unsigned u32x4;
typedef __attribute__((ext_vector_type(2))) unsigned u32x2;

constexpr int kW = 224, kC = 96;
constexpr int ROW_BYTES = kW * kC * 2;
constexpr int FRAME_BYTES = 224 * ROW_BYTES;
constexpr int WMAT_BYTES = 18432;
constexpr int LDS_W = 0;
constexpr int LDS_X = 4 * WMAT_BYTES;
constexpr int XBUF_BYTES = 196 * 192;
constexpr int LDS_R = LDS_X + 2 * XBUF_BYTES;
constexpr int LDS_RHO = LDS_R + 2 * 1024;
constexpr int LDS_CNT = LDS_RHO + 2 * 384;
constexpr int LDS_BYTES = LDS_CNT + 64;
enum { C_GRP0 = 0, C_GRP1 = 1, C_WT = 2, C_WS = 3, C_RHO_T = 4, C_RHO_S = 5 };
constexpr float kEps = 1e-5f;
constexpr float kScaleLog2e = 0.10206207261596577f * 1.4426950408889634f;

DEV float u2f(unsigned u) { return __builtin_bit_cast(float, u); }
DEV unsigned f2u(float f) { return __builtin_bit_cast(unsigned, f); }
DEV int imin(int a, int b) { return a < b ? a : b; }
DEV f32x16 mfma32(u32x4 a, u32x4 b, f32x16 c) {
  return __builtin_amdgcn_mfma_f32_32x32x16_bf16(__builtin_bit_cast(bf16x8, a), __builtin_bit_cast(bf16x8, b), c, 0, 0, 0);
}
DEV f32x4 mfma4(u32x2 a, u32x2 b, f32x4 c) {
  return __builtin_amdgcn_mfma_f32_4x4x4bf16_1k(__builtin_bit_cast(s16x4, a), __builtin_bit_cast(s16x4, b), c, 0, 0, 0);
}
DEV unsigned pk2(float a, float b) {
  f32x2 v = {a, b};
  return __builtin_bit_cast(unsigned, __builtin_convertvector(v, bf16x2));
}
DEV u32x4 pack8(const f32x16& v, int o) {
  u32x4 r;
  r[0] = pk2(v[o + 0], v[o + 1]); r[1] = pk2(v[o + 2], v[o + 3]);
  r[2] = pk2(v[o + 4], v[o + 5]); r[3] = pk2(v[o + 6], v[o + 7]);
  return r;
}
DEV float bf_lo(unsigned u) { return u2f(u << 16); }
DEV float bf_hi(unsigned u) { return u2f(u & 0xffff0000u); }
DEV float xhalf_sum(float x) {
  auto r = __builtin_amdgcn_permlane32_swap(f2u(x), f2u(x), false, false);
  return u2f(r[0]) + u2f(r[1]);
}
DEV float xhalf_max(float x) {
  auto r = __builtin_amdgcn_permlane32_swap(f2u(x), f2u(x), false, false);
  return __builtin_fmaxf(u2f(r[0]), u2f(r[1]));
}
template <int I> DEV float quad_bcast(float x) {
  return u2f(__builtin_amdgcn_update_dpp(0u, f2u(x), I * 0x55, 0xf, 0xf, true));
}
DEV f32x16 zero16() { f32x16 z; for (int i = 0; i < 16; ++i) z[i] = 0.f; return z; }
DEV u32x4 lds_ld16(LAS const char* p) { return *(LAS const u32x4*)p; }
DEV void lds_st16(LAS char* p, u32x4 v) { *(LAS u32x4*)p = v; }

DEV void dma16(const void* sbase, unsigned voff, unsigned lds_dst) {
  unsigned keep;
  asm volatile("s_mov_b32 %0, m0\n\ts_mov_b32 m0, %3\n\ts_nop 0\n\tglobal_load_lds_dwordx4 %1, %2\n\ts_mov_b32 m0, %0"
               : "=&s"(keep) : "v"(voff), "s"(sbase), "s"(lds_dst) : "memory");
}
DEV void dma16_12(const void* sbase, unsigned voff, unsigned lds_dst) {
  unsigned keep; unsigned long long ex;
  asm volatile("s_mov_b32 %0, m0\n\ts_mov_b64 %1, exec\n\ts_mov_b32 m0, %4\n\ts_mov_b64 exec, 0xfff\n\tglobal_load_lds_dwordx4 %2, %3\n\ts_mov_b64 exec, %1\n\ts_mov_b32 m0, %0"
               : "=&s"(keep), "=&s"(ex) : "v"(voff), "s"(sbase), "s"(lds_dst) : "memory");
}
DEV int opaque(int v) { asm volatile("" : "+v"(v)); return v; }
#define STAMP(k) do {} while (0)
DEV void lds_arrive(LAS unsigned* p) {
  const unsigned addr = (unsigned)(size_t)p;
  unsigned long long ex;
  asm volatile("s_waitcnt lgkmcnt(0)\n\ts_mov_b64 %0, exec\n\ts_mov_b64 exec, 1\n\tds_add_u32 %1, %2\n\ts_mov_b64 exec, %0"
               : "=&s"(ex) : "v"(addr), "v"(1u) : "memory");
}
DEV void lds_wait_ge(LAS unsigned* p, unsigned target) {
  const unsigned addr = (unsigned)(size_t)p;
  for (int spin = 0; spin < (1 << 22); ++spin) {
    unsigned v;
    asm volatile("ds_read_b32 %0, %1\n\ts_waitcnt lgkmcnt(0)" : "=&v"(v) : "v"(addr) : "memory");
    if ((unsigned)__builtin_amdgcn_readfirstlane(v) >= target) break;
    __builtin_amdgcn_s_sleep(1);
  }
}
#define LD6(d, sbase, voff, OFF0) \
  asm volatile("global_load_dwordx4 %0, %6, %7 offset:%c8\n\tglobal_load_dwordx4 %1, %6, %7 offset:%c8+32\n\tglobal_load_dwordx4 %2, %6, %7 offset:%c8+64\n\t" \
               "global_load_dwordx4 %3, %6, %7 offset:%c8+96\n\tglobal_load_dwordx4 %4, %6, %7 offset:%c8+128\n\tglobal_load_dwordx4 %5, %6, %7 offset:%c8+160" \
               : "=&v"(d[0]), "=&v"(d[1]), "=&v"(d[2]), "=&v"(d[3]), "=&v"(d[4]), "=&v"(d[5]) : "v"(voff), "s"(sbase), "i"(OFF0) : "memory")
#define PIN6(d) asm volatile("" : "+v"(d[0]), "+v"(d[1]), "+v"(d[2]), "+v"(d[3]), "+v"(d[4]), "+v"(d[5]))
DEV int sigma32(int r) { return (r & 19) | ((r & 4) << 1) | ((r & 8) >> 1); }
DEV void wg_barrier() {
  asm volatile("s_waitcnt lgkmcnt(0)\n\ts_barrier" ::: "memory");
}

DEV float finish_rstd(float sum, float sq) {
  sum = xhalf_sum(sum); sq = xhalf_sum(sq);
  const float m = sum * (1.0f / 96.0f);
  float var = __builtin_fmaf(-m, m, sq * (1.0f / 96.0f));
  var = __builtin_fmaxf(var, 0.f);
  return __builtin_amdgcn_rsqf(var + kEps);
}

DEV void t_tile(LAS char* xb, LAS float* r2, LAS const char* wA, LAS const char* wB, int tile, int lane) {
  const int r = lane & 31, h = lane >> 5, p = r >> 2, t = r & 3;
  const int P = imin(tile * 8 + p, 48);
  const int slot = 49 * t + P;
  const int a = ((P >> 2) + 2 * t) & 3;
  LAS char* ae = xb + 192 * slot + 16 * (h ^ a);
  LAS char* ao = xb + 192 * slot + 16 * ((2 + h) ^ a);
  u32x4 xf[6];
#pragma unroll
  for (int s = 0; s < 6; ++s) xf[s] = lds_ld16(((s & 1) ? ao : ae) + 64 * (s >> 1));

  float sum = 0.f, sq = 0.f;
#pragma unroll
  for (int s = 0; s < 6; ++s)
#pragma unroll
    for (int j = 0; j < 4; ++j) {
      const float lo = bf_lo(xf[s][j]), hi = bf_hi(xf[s][j]);
      sum += lo; sq = __builtin_fmaf(lo, lo, sq);
      sum += hi; sq = __builtin_fmaf(hi, hi, sq);
    }
  const float rs = finish_rstd(sum, sq);

  u32x4 gf[6];
#pragma unroll
  for (int ct = 0; ct < 3; ++ct) {
    f32x16 acc = zero16();
#pragma unroll
    for (int s = 0; s < 6; ++s) acc = mfma32(lds_ld16(wA + (ct * 6 + s) * 1024 + lane * 16), xf[s], acc);
    gf[2 * ct] = pack8(acc, 0); gf[2 * ct + 1] = pack8(acc, 8);
  }
  f32x4 d4 = {0.f, 0.f, 0.f, 0.f};
#pragma unroll
  for (int s = 0; s < 6; ++s) {
    d4 = mfma4(u32x2{xf[s][0], xf[s][1]}, u32x2{gf[s][0], gf[s][1]}, d4);
    d4 = mfma4(u32x2{xf[s][2], xf[s][3]}, u32x2{gf[s][2], gf[s][3]}, d4);
  }
  const float rk0 = quad_bcast<0>(rs), rk1 = quad_bcast<1>(rs), rk2 = quad_bcast<2>(rs), rk3 = quad_bcast<3>(rs);
  const float y0 = xhalf_sum(d4[0]) * rk0, y1 = xhalf_sum(d4[1]) * rk1, y2 = xhalf_sum(d4[2]) * rk2, y3 = xhalf_sum(d4[3]) * rk3;
  const float mx = __builtin_fmaxf(__builtin_fmaxf(y0, y1), __builtin_fmaxf(y2, y3));
  const float nb = -mx * rs;
  const float e0 = __builtin_amdgcn_exp2f(__builtin_fmaf(y0, rs, nb)), e1 = __builtin_amdgcn_exp2f(__builtin_fmaf(y1, rs, nb));
  const float e2 = __builtin_amdgcn_exp2f(__builtin_fmaf(y2, rs, nb)), e3 = __builtin_amdgcn_exp2f(__builtin_fmaf(y3, rs, nb));
  const float inv = __builtin_amdgcn_rcpf((e0 + e1) + (e2 + e3));
  const unsigned p01 = pk2(e0 * (rk0 * inv), e1 * (rk1 * inv)), p23 = pk2(e2 * (rk2 * inv), e3 * (rk3 * inv));
  u32x4 pf[2];
#pragma unroll
  for (int g = 0; g < 4; ++g) {
    const bool on = (p == 2 * g + h);
    pf[g >> 1][2 * (g & 1)] = on ? p01 : 0u;
    pf[g >> 1][2 * (g & 1) + 1] = on ? p23 : 0u;
  }
  float sum2 = 0.f, sq2 = 0.f;
  u32x4 of[6];
#pragma unroll
  for (int ct = 0; ct < 3; ++ct) {
    f32x16 u = zero16();
#pragma unroll
    for (int s = 0; s < 6; ++s) u = mfma32(xf[s], lds_ld16(wB + (ct * 6 + s) * 1024 + lane * 16), u);
    const u32x4 u0 = pack8(u, 0), u1 = pack8(u, 8);
    f32x16 acc;
#pragma unroll
    for (int i = 0; i < 16; ++i) {
      const unsigned q = xf[2 * ct + (i >> 3)][(i & 7) >> 1];
      acc[i] = (i & 1) ? bf_hi(q) : bf_lo(q);
    }
    acc = mfma32(u0, pf[0], acc);
    acc = mfma32(u1, pf[1], acc);
    of[2 * ct] = pack8(acc, 0); of[2 * ct + 1] = pack8(acc, 8);
#pragma unroll
    for (int i = 0; i < 16; ++i) { sum2 += acc[i]; sq2 = __builtin_fmaf(acc[i], acc[i], sq2); }
  }
  const float rs2 = finish_rstd(sum2, sq2);
#pragma unroll
  for (int s = 0; s < 6; ++s) lds_st16(((s & 1) ? ao : ae) + 64 * (s >> 1), of[s]);
  r2[t * 64 + P] = rs2;
}

DEV void s_qtile(const u32x4 (&hq)[6], const u32x4 (&h0)[6], const u32x4 (&h1)[6], const u32x4 (&uf)[2][3][2],
                 LAS const char* wA, LAS const float* r2t, float rq, int lane, __amdgpu_buffer_rsrc_t orsrc, unsigned ooff) {
  const int h = lane >> 5;
  u32x4 gf[6];
#pragma unroll
  for (int ct = 0; ct < 3; ++ct) {
    f32x16 acc = zero16();
#pragma unroll
    for (int s = 0; s < 6; ++s) acc = mfma32(lds_ld16(wA + (ct * 6 + s) * 1024 + lane * 16), hq[s], acc);
    gf[2 * ct] = pack8(acc, 0); gf[2 * ct + 1] = pack8(acc, 8);
  }
  f32x16 sA = zero16(), sB = zero16();
#pragma unroll
  for (int s = 0; s < 6; ++s) sA = mfma32(h0[s], gf[s], sA);
#pragma unroll
  for (int s = 0; s < 6; ++s) sB = mfma32(h1[s], gf[s], sB);
  float rkA[16], rkB[9];
#pragma unroll
  for (int q = 0; q < 4; ++q) {
    const f32x4 v = *(LAS const f32x4*)(r2t + 8 * q + 4 * h);
    rkA[4 * q] = v[0]; rkA[4 * q + 1] = v[1]; rkA[4 * q + 2] = v[2]; rkA[4 * q + 3] = v[3];
  }
#pragma unroll
  for (int q = 0; q < 2; ++q) {
    const f32x4 v = *(LAS const f32x4*)(r2t + 32 + 8 * q + 4 * h);
    rkB[4 * q] = v[0]; rkB[4 * q + 1] = v[1]; rkB[4 * q + 2] = v[2]; rkB[4 * q + 3] = v[3];
  }
  rkB[8] = r2t[48];
  float yA[16], yB[9];
#pragma unroll
  for (int i = 0; i < 16; ++i) yA[i] = sA[i] * rkA[i];
#pragma unroll
  for (int i = 0; i < 9; ++i) yB[i] = sB[i] * rkB[i];
  yB[8] = h ? -__builtin_inff() : yB[8];
  float mx = yB[8];
#pragma unroll
  for (int i = 0; i < 16; i += 2) mx = __builtin_fmaxf(__builtin_fmaxf(mx, yA[i]), yA[i + 1]);
#pragma unroll
  for (int i = 0; i < 8; i += 2) mx = __builtin_fmaxf(__builtin_fmaxf(mx, yB[i]), yB[i + 1]);
  mx = xhalf_max(mx);
  const float nb = -mx * rq;
  float l = 0.f;
#pragma unroll
  for (int i = 0; i < 16; ++i) { yA[i] = __builtin_amdgcn_exp2f(__builtin_fmaf(yA[i], rq, nb)); l += yA[i]; yA[i] *= rkA[i]; }
#pragma unroll
  for (int i = 0; i < 9; ++i) { yB[i] = __builtin_amdgcn_exp2f(__builtin_fmaf(yB[i], rq, nb)); l += yB[i]; yB[i] *= rkB[i]; }
  const float inv = __builtin_amdgcn_rcpf(xhalf_sum(l));
  u32x4 pf[4];
#pragma unroll
  for (int j = 0; j < 4; ++j) {
    pf[0][j] = pk2(yA[2 * j], yA[2 * j + 1]);
    pf[1][j] = pk2(yA[8 + 2 * j], yA[8 + 2 * j + 1]);
    pf[2][j] = pk2(yB[2 * j], yB[2 * j + 1]);
  }
  pf[3][0] = pk2(yB[8], 0.f); pf[3][1] = 0u; pf[3][2] = 0u; pf[3][3] = 0u;
#pragma unroll
  for (int ct = 0; ct < 3; ++ct) {
    f32x16 o = zero16();
    o = mfma32(uf[0][ct][0], pf[0], o);
    o = mfma32(uf[0][ct][1], pf[1], o);
    o = mfma32(uf[1][ct][0], pf[2], o);
    o = mfma32(uf[1][ct][1], pf[3], o);
#pragma unroll
    for (int i = 0; i < 16; ++i) {
      const unsigned q = hq[2 * ct + (i >> 3)][(i & 7) >> 1];
      o[i] = __builtin_fmaf(o[i], inv, (i & 1) ? bf_hi(q) : bf_lo(q));
    }
    __builtin_amdgcn_raw_buffer_store_b128(pack8(o, 0), orsrc, ooff + (2 * ct) * 32, 0, 0);
    __builtin_amdgcn_raw_buffer_store_b128(pack8(o, 8), orsrc, ooff + (2 * ct + 1) * 32, 0, 0);
  }
}

struct PrepA { u32x4 q[3][6], k[3][6]; };
DEV void prep_a_load(PrepA& a, const char* wqkv, int jt, int lane) {
  const int r = lane & 31, h = lane >> 5;
#pragma unroll
  for (int d = 0; d < 3; ++d) {
    int it = jt + d; it = it >= 3 ? it - 3 : it;
    const unsigned voff = (32 * it + sigma32(r)) * 576 + 16 * h;
    LD6(a.q[d], wqkv, voff, 0);
    LD6(a.k[d], wqkv, voff, 192);
  }
}
DEV void prep_a_compute(PrepA& a, LAS char* dst, LAS float* rho, LAS unsigned* cnt, int jt, int lane) {
  const int r = lane & 31, h = lane >> 5;
#pragma unroll
  for (int d = 0; d < 3; ++d) { PIN6(a.q[d]); PIN6(a.k[d]); }
  f32x16 R = zero16();
#pragma unroll
  for (int d = 0; d < 3; ++d)
#pragma unroll
    for (int s = 0; s < 6; ++s) R = mfma32(a.k[d][s], a.q[0][s], R);
  f32x16 D[3];
#pragma unroll
  for (int d = 0; d < 3; ++d) {
    D[d] = zero16();
#pragma unroll
    for (int s = 0; s < 6; ++s) D[d] = mfma32(a.q[d][s], a.k[0][s], D[d]);
  }
  float rsum = 0.f;
#pragma unroll
  for (int i = 0; i < 16; ++i) rsum += R[i];
  rsum = xhalf_sum(rsum);
  rho[32 * jt + sigma32(r)] = rsum;
  lds_arrive(cnt);
  float ks = 0.f;
#pragma unroll
  for (int d = 0; d < 3; ++d)
#pragma unroll
    for (int i = 0; i < 16; ++i) ks += D[d][i];
  ks = xhalf_sum(ks);
  lds_wait_ge(cnt, 3);
  float rv[3][16], tau = 0.f;
#pragma unroll
  for (int d = 0; d < 3; ++d) {
    int it = jt + d; it = it >= 3 ? it - 3 : it;
#pragma unroll
    for (int u = 0; u < 4; ++u) {
      const f32x4 v = *(LAS const f32x4*)(rho + 32 * it + 16 * (u >> 1) + 8 * h + 4 * (u & 1));
#pragma unroll
      for (int e = 0; e < 4; ++e) { rv[d][4 * u + e] = v[e]; tau += v[e]; }
    }
  }
  tau = xhalf_sum(tau);
  const float c0 = (tau * (1.0f / 96.0f) - ks) * (1.0f / 96.0f);
#pragma unroll
  for (int d = 0; d < 3; ++d) {
    int it = jt + d; it = it >= 3 ? it - 3 : it;
#pragma unroll
    for (int i = 0; i < 16; ++i) D[d][i] = (__builtin_fmaf(rv[d][i], -1.0f / 96.0f, D[d][i]) + c0) * kScaleLog2e;
    lds_st16(dst + (jt * 6 + 2 * it) * 1024 + lane * 16, pack8(D[d], 0));
    lds_st16(dst + (jt * 6 + 2 * it + 1) * 1024 + lane * 16, pack8(D[d], 8));
  }
}
struct PrepB { u32x4 v[3][6]; };
DEV void prep_b_load(PrepB& b, const char* wqkv, const char* wo, unsigned lds_dst, int lane) {
  const int r = lane & 31, h = lane >> 5;
#pragma unroll
  for (int i = 0; i < 18; ++i) dma16(wo, i * 1024 + lane * 16, lds_dst + i * 1024);
#pragma unroll
  for (int it = 0; it < 3; ++it) {
    const unsigned voff = (32 * it + sigma32(r)) * 576 + 16 * h;
    LD6(b.v[it], wqkv, voff, 384);
  }
}
DEV void prep_b_compute(PrepB& b, LAS char* dst, int lane) {
  const int h = lane >> 5, blk = (lane >> 4) & 1, q = (lane & 15) >> 2, p = lane & 3;
  const int psw = ((p & 1) << 1) | (p >> 1);
#pragma unroll
  for (int it = 0; it < 3; ++it) PIN6(b.v[it]);
  u32x4 o[3][6];
  LAS char* tb = dst + (8 * h + q) * 192 + (16 * blk + 4 * psw) * 2;
#pragma unroll
  for (int jt = 0; jt < 3; ++jt)
#pragma unroll
    for (int s = 0; s < 6; ++s) {
      const s16x4 lo = __builtin_amdgcn_ds_read_tr16_b64_v4i16((LAS s16x4*)(tb + (16 * s) * 192 + jt * 64));
      const s16x4 hi = __builtin_amdgcn_ds_read_tr16_b64_v4i16((LAS s16x4*)(tb + (16 * s + 4) * 192 + jt * 64));
      const u32x2 l2 = __builtin_bit_cast(u32x2, lo), h2 = __builtin_bit_cast(u32x2, hi);
      o[jt][s] = u32x4{l2[0], l2[1], h2[0], h2[1]};
    }
  asm volatile("s_waitcnt lgkmcnt(0)" ::: "memory");
#pragma unroll
  for (int jt = 0; jt < 3; ++jt) PIN6(o[jt]);
#pragma unroll
  for (int jt = 0; jt < 3; ++jt) {
    f32x16 D[3];
    float ks = 0.f;
#pragma unroll
    for (int it = 0; it < 3; ++it) {
      D[it] = zero16();
#pragma unroll
      for (int s = 0; s < 6; ++s) D[it] = mfma32(b.v[it][s], o[jt][s], D[it]);
#pragma unroll
      for (int i = 0; i < 16; ++i) ks += D[it][i];
    }
    ks = xhalf_sum(ks) * (1.0f / 96.0f);
#pragma unroll
    for (int it = 0; it < 3; ++it) {
#pragma unroll
      for (int i = 0; i < 16; ++i) D[it][i] -= ks;
      lds_st16(dst + (jt * 6 + 2 * it) * 1024 + lane * 16, pack8(D[it], 0));
      lds_st16(dst + (jt * 6 + 2 * it + 1) * 1024 + lane * 16, pack8(D[it], 8));
    }
  }
}

DEV void dma_frame(const char* x, int col, int t, int lane, unsigned lds_dst) {
  const int wh = col >> 5, ww = col & 31;
  const char* base = x + (size_t)t * FRAME_BYTES + (size_t)(wh * 7) * ROW_BYTES + ww * (7 * 192);
#pragma unroll
  for (int i = 0; i < 10; ++i) {
    const int u = i * 64 + lane;
    const int P = u / 12, f = u - P * 12;
    const int a = ((P >> 2) + 2 * t) & 3;
    const int row = P / 7, cc = P - row * 7;
    const unsigned voff = row * ROW_BYTES + cc * 192 + ((f ^ a) << 4);
    if (i < 9) dma16(base, voff, lds_dst + i * 1024);
    else dma16_12(base, voff, lds_dst + i * 1024);
  }
}

__global__ NOPK __launch_bounds__(512, 2) void swin_main(const char* __restrict__ x, char* __restrict__ out, const char* __restrict__ wqkv_t,
                                                         const char* __restrict__ wo_t, const char* __restrict__ wqkv_s, const char* __restrict__ wo_s
) {
  extern __shared__ __attribute__((aligned(16))) char smem_[];
  LAS char* smem = (LAS char*)smem_;
  const int lane = __builtin_amdgcn_workitem_id_x() & 63;
  const int w = __builtin_amdgcn_readfirstlane(__builtin_amdgcn_workitem_id_x() >> 6);
  const int g = w >> 2, t = w & 3;
  STAMP(0);
  const __amdgpu_buffer_rsrc_t orsrc = __builtin_amdgcn_make_buffer_rsrc(out, 0, 4 * FRAME_BYTES, 0x00020000);
  const unsigned lds0 = __builtin_amdgcn_readfirstlane((unsigned)(size_t)smem);
  LAS char* xb = smem + LDS_X + g * XBUF_BYTES;
  LAS float* r2 = (LAS float*)(smem + LDS_R + g * 1024);
  LAS unsigned* cnt = (LAS unsigned*)(smem + LDS_CNT);
  const int bid = __builtin_amdgcn_workgroup_id_x();

  {
    const char* wqkv = g ? wqkv_s : wqkv_t;
    const char* wo = g ? wo_s : wo_t;
    const unsigned xdst = lds0 + LDS_X + g * XBUF_BYTES + t * (49 * 192);
    const int role = g ? (t == 0 ? 3 : t - 1) : t;
    if (role < 3) {
      PrepA a;
      prep_a_load(a, wqkv, role, lane);
      dma_frame(x, bid * 4 + g, t, lane, xdst);
      if (__builtin_amdgcn_workitem_id_x() < 16) cnt[__builtin_amdgcn_workitem_id_x()] = 0u;
      wg_barrier();
      asm volatile("s_waitcnt vmcnt(10)" ::: "memory");
      prep_a_compute(a, smem + LDS_W + (2 * g) * WMAT_BYTES, (LAS float*)(smem + LDS_RHO) + 96 * g, cnt + C_RHO_T + g, role, lane);
    } else {
      PrepB b;
      prep_b_load(b, wqkv, wo, lds0 + LDS_W + (2 * g + 1) * WMAT_BYTES, lane);
      dma_frame(x, bid * 4 + g, t, lane, xdst);
      wg_barrier();
      asm volatile("s_waitcnt vmcnt(10)" ::: "memory");
      prep_b_compute(b, smem + LDS_W + (2 * g + 1) * WMAT_BYTES, lane);
    }
    lds_arrive(cnt + C_WT + g);
  }
  STAMP(13);

#pragma unroll 1
  for (int round = 0; round < 2; ++round) {
    const int col = bid * 4 + round * 2 + g;
    if (round == 0) asm volatile("s_waitcnt vmcnt(0)" ::: "memory");
    else asm volatile("s_waitcnt vmcnt(12)" ::: "memory");
    STAMP(1 + round * 6);
    lds_arrive(cnt + g);
    lds_wait_ge(cnt + g, 8 * round + 4);
    if (round == 0) lds_wait_ge(cnt + C_WT, 4);
    STAMP(2 + round * 6);
#pragma unroll 1
    for (int tt = 0; tt < 2; ++tt) {
      const int tile = t + 4 * tt;
      if (tile < 7) t_tile(xb, r2, smem + LDS_W, smem + LDS_W + WMAT_BYTES, tile, opaque(lane));
    }
    STAMP(3 + round * 6);
    lds_arrive(cnt + g);
    lds_wait_ge(cnt + g, 8 * round + 8);
    if (round == 0) lds_wait_ge(cnt + C_WS, 4);
    STAMP(4 + round * 6);
    const int lane_s = opaque(lane);
#define lane lane_s
    const int r = lane & 31, h = lane >> 5;
    u32x4 h0[6], h1[6];
    {
      const int j0 = r, j1 = imin(32 + r, 48);
      const int a0 = ((j0 >> 2) + 2 * t) & 3, a1 = ((j1 >> 2) + 2 * t) & 3;
      LAS const char* b0 = xb + 192 * (49 * t + j0);
      LAS const char* b1 = xb + 192 * (49 * t + j1);
#pragma unroll
      for (int s = 0; s < 6; ++s) {
        h0[s] = lds_ld16(b0 + 16 * ((2 * (s & 1) + h) ^ a0) + 64 * (s >> 1));
        h1[s] = lds_ld16(b1 + 16 * ((2 * (s & 1) + h) ^ a1) + 64 * (s >> 1));
      }
    }
    LAS const float* r2t = r2 + t * 64;
    const float rq0 = r2t[r], rq1 = r2t[imin(32 + r, 48)];
    if (round == 0) {
      asm volatile("s_waitcnt lgkmcnt(0)" ::: "memory");
      asm volatile("" : "+v"(h0[0]), "+v"(h0[1]), "+v"(h0[2]), "+v"(h0[3]), "+v"(h0[4]), "+v"(h0[5]));
      asm volatile("" : "+v"(h1[0]), "+v"(h1[1]), "+v"(h1[2]), "+v"(h1[3]), "+v"(h1[4]), "+v"(h1[5]));
      dma_frame(x, col + 2, t, opaque(lane), lds0 + LDS_X + g * XBUF_BYTES + t * (49 * 192));
    }
    STAMP(5 + round * 6);
    LAS const char* wA = smem + LDS_W + 2 * WMAT_BYTES;
    LAS const char* wB = smem + LDS_W + 3 * WMAT_BYTES;
    u32x4 uf[2][3][2];
#pragma unroll
    for (int ct = 0; ct < 3; ++ct) {
      f32x16 u0 = zero16(), u1 = zero16();
#pragma unroll
      for (int s = 0; s < 6; ++s) {
        const u32x4 wv = lds_ld16(wB + (ct * 6 + s) * 1024 + lane * 16);
        u0 = mfma32(h0[s], wv, u0);
        u1 = mfma32(h1[s], wv, u1);
      }
      uf[0][ct][0] = pack8(u0, 0); uf[0][ct][1] = pack8(u0, 8);
      uf[1][ct][0] = pack8(u1, 0); uf[1][ct][1] = pack8(u1, 8);
    }
    const int wh = col >> 5, ww = col & 31;
    const unsigned obase = t * FRAME_BYTES + (wh * 7) * ROW_BYTES + ww * (7 * 192) + h * 16;
    {
      const int j = r, row = j / 7, cc = j - row * 7;
      s_qtile(h0, h0, h1, uf, wA, r2t, rq0, lane, orsrc, obase + row * ROW_BYTES + cc * 192);
    }
    {
      const int j = imin(32 + r, 48), row = j / 7, cc = j - row * 7;
      s_qtile(h1, h0, h1, uf, wA, r2t, rq1, lane, orsrc, r <= 16 ? obase + row * ROW_BYTES + cc * 192 : 0x80000000u);
    }
    STAMP(6 + round * 6);
#undef lane
  }
}

extern "C" void kernel_launch(void* const* d_in, const int* in_sizes, int n_in, void* d_out, int out_size, void* d_ws,
                              size_t ws_size, hipStream_t stream) {
  (void)in_sizes; (void)n_in; (void)out_size; (void)ws_size; (void)d_ws;
  (void)hipFuncSetAttribute((const void*)swin_main, hipFuncAttributeMaxDynamicSharedMemorySize, LDS_BYTES);
  swin_main<<<256, 512, LDS_BYTES, stream>>>((const char*)d_in[0], (char*)d_out, (const char*)d_in[1], (const char*)d_in[2],
                                             (const char*)d_in[3], (const char*)d_in[4]
  );
}
```

```cpp
#include <hip/hip_runtime.h>
#include <hip/hip_bf16.h>
#include <stdint.h>

#if defined(__HIP_DEVICE_COMPILE__)
#define NOPK __attribute__((target("no-packed-fp32-ops")))
#else
#define NOPK
#endif
#define DEV __device__ __forceinline__ NOPK
#define LAS __attribute__((address_space(3)))

typedef __attribute__((ext_vector_type(8))) __bf16 bf16x8;
typedef __attribute__((ext_vector_type(2))) __bf16 bf16x2;
typedef __attribute__((ext_vector_type(4))) short s16x4;
typedef __attribute__((ext_vector_type(16))) float f32x16;
typedef __attribute__((ext_vector_type(4))) float f32x4;
typedef __attribute__((ext_vector_type(2))) float f32x2;
typedef __attribute__((ext_vector_type(4))) unsigned u32x4;
typedef __attribute__((ext_vector_type(2))) unsigned u32x2;

constexpr int kW = 224, kC = 96;
constexpr int ROW_BYTES = kW * kC * 2;
constexpr int FRAME_BYTES = 224 * ROW_BYTES;
constexpr int WMAT_BYTES = 18432;
constexpr int LDS_W = 0;
constexpr int LDS_X = 4 * WMAT_BYTES;
constexpr int XBUF_BYTES = 196 * 192;
constexpr int LDS_R = LDS_X + 2 * XBUF_BYTES;
constexpr int LDS_RHO = LDS_R + 2 * 1024;
constexpr int LDS_CNT = LDS_RHO + 2 * 384;
constexpr int LDS_BYTES = LDS_CNT + 64;
enum { C_GRP0 = 0, C_GRP1 = 1, C_WT = 2, C_WS = 3, C_RHO_T = 4, C_RHO_S = 5 };
constexpr float kEps = 1e-5f;
constexpr float kScaleLog2e = 0.10206207261596577f * 1.4426950408889634f;

DEV float u2f(unsigned u) { return __builtin_bit_cast(float, u); }
DEV unsigned f2u(float f) { return __builtin_bit_cast(unsigned, f); }
DEV int imin(int a, int b) { return a < b ? a : b; }
DEV f32x16 mfma32(u32x4 a, u32x4 b, f32x16 c) {
  return __builtin_amdgcn_mfma_f32_32x32x16_bf16(__builtin_bit_cast(bf16x8, a), __builtin_bit_cast(bf16x8, b), c, 0, 0, 0);
}
DEV f32x4 mfma4(u32x2 a, u32x2 b, f32x4 c) {
  return __builtin_amdgcn_mfma_f32_4x4x4bf16_1k(__builtin_bit_cast(s16x4, a), __builtin_bit_cast(s16x4, b), c, 0, 0, 0);
}
DEV unsigned pk2(float a, float b) {
  f32x2 v = {a, b};
  return __builtin_bit_cast(unsigned, __builtin_convertvector(v, bf16x2));
}
DEV u32x4 pack8(const f32x16& v, int o) {
  u32x4 r;
  r[0] = pk2(v[o + 0], v[o + 1]); r[1] = pk2(v[o + 2], v[o + 3]);
  r[2] = pk2(v[o + 4], v[o + 5]); r[3] = pk2(v[o + 6], v[o + 7]);
  return r;
}
DEV float bf_lo(unsigned u) { return u2f(u << 16); }
DEV float bf_hi(unsigned u) { return u2f(u & 0xffff0000u); }
DEV float xhalf_sum(float x) {
  auto r = __builtin_amdgcn_permlane32_swap(f2u(x), f2u(x), false, false);
  return u2f(r[0]) + u2f(r[1]);
}
DEV float xhalf_max(float x) {
  auto r = __builtin_amdgcn_permlane32_swap(f2u(x), f2u(x), false, false);
  return __builtin_fmaxf(u2f(r[0]), u2f(r[1]));
}
template <int I> DEV float quad_bcast(float x) {
  return u2f(__builtin_amdgcn_update_dpp(0u, f2u(x), I * 0x55, 0xf, 0xf, true));
}
DEV f32x16 zero16() { f32x16 z; for (int i = 0; i < 16; ++i) z[i] = 0.f; return z; }
DEV u32x4 lds_ld16(LAS const char* p) { return *(LAS const u32x4*)p; }
DEV void lds_st16(LAS char* p, u32x4 v) { *(LAS u32x4*)p = v; }

DEV const char* uptr(const void* p) {
  const unsigned long long v = (unsigned long long)p;
  const unsigned lo = __builtin_amdgcn_readfirstlane((unsigned)v), hi = __builtin_amdgcn_readfirstlane((unsigned)(v >> 32));
  return (const char*)(((unsigned long long)hi << 32) | lo);
}
DEV void dma16(const void* sbase, unsigned voff, unsigned lds_dst) {
  unsigned keep;
  lds_dst = __builtin_amdgcn_readfirstlane(lds_dst); sbase = uptr(sbase);
  asm volatile("s_mov_b32 %0, m0\n\ts_mov_b32 m0, %3\n\ts_nop 0\n\tglobal_load_lds_dwordx4 %1, %2\n\ts_mov_b32 m0, %0"
               : "=&s"(keep) : "v"(voff), "s"(sbase), "s"(lds_dst) : "memory");
}
DEV void dma16_12(const void* sbase, unsigned voff, unsigned lds_dst) {
  unsigned keep; unsigned long long ex;
  lds_dst = __builtin_amdgcn_readfirstlane(lds_dst); sbase = uptr(sbase);
  asm volatile("s_mov_b32 %0, m0\n\ts_mov_b64 %1, exec\n\ts_mov_b32 m0, %4\n\ts_mov_b64 exec, 0xfff\n\tglobal_load_lds_dwordx4 %2, %3\n\ts_mov_b64 exec, %1\n\ts_mov_b32 m0, %0"
               : "=&s"(keep), "=&s"(ex) : "v"(voff), "s"(sbase), "s"(lds_dst) : "memory");
}
DEV int opaque(int v) { asm volatile("" : "+v"(v)); return v; }
#define STAMP(k) do {} while (0)
#define RSTAMP(k) do {} while (0)
DEV void lds_arrive(LAS unsigned* p) {
  const unsigned addr = (unsigned)(size_t)p;
  unsigned long long ex;
  asm volatile("s_waitcnt lgkmcnt(0)\n\ts_mov_b64 %0, exec\n\ts_mov_b64 exec, 1\n\tds_add_u32 %1, %2\n\ts_mov_b64 exec, %0"
               : "=&s"(ex) : "v"(addr), "v"(1u) : "memory");
}
DEV void lds_wait_ge(LAS unsigned* p, unsigned target) {
  const unsigned addr = (unsigned)(size_t)p;
  for (int spin = 0; spin < (1 << 22); ++spin) {
    unsigned v;
    asm volatile("ds_read_b32 %0, %1\n\ts_waitcnt lgkmcnt(0)" : "=&v"(v) : "v"(addr) : "memory");
    if ((unsigned)__builtin_amdgcn_readfirstlane(v) >= target) break;
    __builtin_amdgcn_s_sleep(1);
  }
}
#define LD6(d, sbase, voff, OFF0) \
  asm volatile("global_load_dwordx4 %0, %6, %7 offset:%c8\n\tglobal_load_dwordx4 %1, %6, %7 offset:%c8+32\n\tglobal_load_dwordx4 %2, %6, %7 offset:%c8+64\n\t" \
               "global_load_dwordx4 %3, %6, %7 offset:%c8+96\n\tglobal_load_dwordx4 %4, %6, %7 offset:%c8+128\n\tglobal_load_dwordx4 %5, %6, %7 offset:%c8+160" \
               : "=&v"(d[0]), "=&v"(d[1]), "=&v"(d[2]), "=&v"(d[3]), "=&v"(d[4]), "=&v"(d[5]) : "v"(voff), "s"(sbase), "i"(OFF0) : "memory")
#define PIN6(d) asm volatile("" : "+v"(d[0]), "+v"(d[1]), "+v"(d[2]), "+v"(d[3]), "+v"(d[4]), "+v"(d[5]))
DEV int sigma32(int r) { return (r & 19) | ((r & 4) << 1) | ((r & 8) >> 1); }
DEV void wg_barrier() {
  asm volatile("s_waitcnt lgkmcnt(0)\n\ts_barrier" ::: "memory");
}

DEV float finish_rstd(float sum, float sq) {
  sum = xhalf_sum(sum); sq = xhalf_sum(sq);
  const float m = sum * (1.0f / 96.0f);
  float var = __builtin_fmaf(-m, m, sq * (1.0f / 96.0f));
  var = __builtin_fmaxf(var, 0.f);
  return __builtin_amdgcn_rsqf(var + kEps);
}

DEV void t_tile(LAS char* xb, LAS float* r2, LAS const char* wA, LAS const char* wB, int tile, int lane) {
  const int r = lane & 31, h = lane >> 5, p = r >> 2, t = r & 3;
  const int P = imin(tile * 8 + p, 48);
  const int slot = 49 * t + P;
  const int a = ((P >> 2) + 2 * t) & 3;
  LAS char* ae = xb + 192 * slot + 16 * (h ^ a);
  LAS char* ao = xb + 192 * slot + 16 * ((2 + h) ^ a);
  u32x4 xf[6];
#pragma unroll
  for (int s = 0; s < 6; ++s) xf[s] = lds_ld16(((s & 1) ? ao : ae) + 64 * (s >> 1));

  float sum = 0.f, sq = 0.f;
#pragma unroll
  for (int s = 0; s < 6; ++s)
#pragma unroll
    for (int j = 0; j < 4; ++j) {
      const float lo = bf_lo(xf[s][j]), hi = bf_hi(xf[s][j]);
      sum += lo; sq = __builtin_fmaf(lo, lo, sq);
      sum += hi; sq = __builtin_fmaf(hi, hi, sq);
    }
  const float rs = finish_rstd(sum, sq);

  u32x4 gf[6];
#pragma unroll
  for (int ct = 0; ct < 3; ++ct) {
    f32x16 acc = zero16();
#pragma unroll
    for (int s = 0; s < 6; ++s) acc = mfma32(lds_ld16(wA + (ct * 6 + s) * 1024 + lane * 16), xf[s], acc);
    gf[2 * ct] = pack8(acc, 0); gf[2 * ct + 1] = pack8(acc, 8);
  }
  f32x4 d4 = {0.f, 0.f, 0.f, 0.f};
#pragma unroll
  for (int s = 0; s < 6; ++s) {
    d4 = mfma4(u32x2{xf[s][0], xf[s][1]}, u32x2{gf[s][0], gf[s][1]}, d4);
    d4 = mfma4(u32x2{xf[s][2], xf[s][3]}, u32x2{gf[s][2], gf[s][3]}, d4);
  }
  const float rk0 = quad_bcast<0>(rs), rk1 = quad_bcast<1>(rs), rk2 = quad_bcast<2>(rs), rk3 = quad_bcast<3>(rs);
  const float y0 = xhalf_sum(d4[0]) * rk0, y1 = xhalf_sum(d4[1]) * rk1, y2 = xhalf_sum(d4[2]) * rk2, y3 = xhalf_sum(d4[3]) * rk3;
  const float mx = __builtin_fmaxf(__builtin_fmaxf(y0, y1), __builtin_fmaxf(y2, y3));
  const float nb = -mx * rs;
  const float e0 = __builtin_amdgcn_exp2f(__builtin_fmaf(y0, rs, nb)), e1 = __builtin_amdgcn_exp2f(__builtin_fmaf(y1, rs, nb));
  const float e2 = __builtin_amdgcn_exp2f(__builtin_fmaf(y2, rs, nb)), e3 = __builtin_amdgcn_exp2f(__builtin_fmaf(y3, rs, nb));
  const float inv = __builtin_amdgcn_rcpf((e0 + e1) + (e2 + e3));
  const unsigned p01 = pk2(e0 * (rk0 * inv), e1 * (rk1 * inv)), p23 = pk2(e2 * (rk2 * inv), e3 * (rk3 * inv));
  u32x4 pf[2];
#pragma unroll
  for (int g = 0; g < 4; ++g) {
    const bool on = (p == 2 * g + h);
    pf[g >> 1][2 * (g & 1)] = on ? p01 : 0u;
    pf[g >> 1][2 * (g & 1) + 1] = on ? p23 : 0u;
  }
  float sum2 = 0.f, sq2 = 0.f;
  u32x4 of[6];
#pragma unroll
  for (int ct = 0; ct < 3; ++ct) {
    f32x16 u = zero16();
#pragma unroll
    for (int s = 0; s < 6; ++s) u = mfma32(xf[s], lds_ld16(wB + (ct * 6 + s) * 1024 + lane * 16), u);
    const u32x4 u0 = pack8(u, 0), u1 = pack8(u, 8);
    f32x16 acc;
#pragma unroll
    for (int i = 0; i < 16; ++i) {
      const unsigned q = xf[2 * ct + (i >> 3)][(i & 7) >> 1];
      acc[i] = (i & 1) ? bf_hi(q) : bf_lo(q);
    }
    acc = mfma32(u0, pf[0], acc);
    acc = mfma32(u1, pf[1], acc);
    of[2 * ct] = pack8(acc, 0); of[2 * ct + 1] = pack8(acc, 8);
#pragma unroll
    for (int i = 0; i < 16; ++i) { sum2 += acc[i]; sq2 = __builtin_fmaf(acc[i], acc[i], sq2); }
  }
  const float rs2 = finish_rstd(sum2, sq2);
#pragma unroll
  for (int s = 0; s < 6; ++s) lds_st16(((s & 1) ? ao : ae) + 64 * (s >> 1), of[s]);
  r2[t * 64 + P] = rs2;
}

DEV void s_qtile(const u32x4 (&hq)[6], const u32x4 (&h0)[6], const u32x4 (&h1)[6], const u32x4 (&uf)[2][3][2],
                 LAS const char* wA, LAS const float* r2t, float rq, int lane, __amdgpu_buffer_rsrc_t orsrc, unsigned ooff) {
  const int h = lane >> 5;
  u32x4 gf[6];
#pragma unroll
  for (int ct = 0; ct < 3; ++ct) {
    f32x16 acc = zero16();
#pragma unroll
    for (int s = 0; s < 6; ++s) acc = mfma32(lds_ld16(wA + (ct * 6 + s) * 1024 + lane * 16), hq[s], acc);
    gf[2 * ct] = pack8(acc, 0); gf[2 * ct + 1] = pack8(acc, 8);
  }
  f32x16 sA = zero16(), sB = zero16();
#pragma unroll
  for (int s = 0; s < 6; ++s) sA = mfma32(h0[s], gf[s], sA);
#pragma unroll
  for (int s = 0; s < 6; ++s) sB = mfma32(h1[s], gf[s], sB);
  float rkA[16], rkB[9];
#pragma unroll
  for (int q = 0; q < 4; ++q) {
    const f32x4 v = *(LAS const f32x4*)(r2t + 8 * q + 4 * h);
    rkA[4 * q] = v[0]; rkA[4 * q + 1] = v[1]; rkA[4 * q + 2] = v[2]; rkA[4 * q + 3] = v[3];
  }
#pragma unroll
  for (int q = 0; q < 2; ++q) {
    const f32x4 v = *(LAS const f32x4*)(r2t + 32 + 8 * q + 4 * h);
    rkB[4 * q] = v[0]; rkB[4 * q + 1] = v[1]; rkB[4 * q + 2] = v[2]; rkB[4 * q + 3] = v[3];
  }
  rkB[8] = r2t[48];
  float yA[16], yB[9];
#pragma unroll
  for (int i = 0; i < 16; ++i) yA[i] = sA[i] * rkA[i];
#pragma unroll
  for (int i = 0; i < 9; ++i) yB[i] = sB[i] * rkB[i];
  yB[8] = h ? -__builtin_inff() : yB[8];
  float mx = yB[8];
#pragma unroll
  for (int i = 0; i < 16; i += 2) mx = __builtin_fmaxf(__builtin_fmaxf(mx, yA[i]), yA[i + 1]);
#pragma unroll
  for (int i = 0; i < 8; i += 2) mx = __builtin_fmaxf(__builtin_fmaxf(mx, yB[i]), yB[i + 1]);
  mx = xhalf_max(mx);
  const float nb = -mx * rq;
  float l = 0.f;
#pragma unroll
  for (int i = 0; i < 16; ++i) { yA[i] = __builtin_amdgcn_exp2f(__builtin_fmaf(yA[i], rq, nb)); l += yA[i]; yA[i] *= rkA[i]; }
#pragma unroll
  for (int i = 0; i < 9; ++i) { yB[i] = __builtin_amdgcn_exp2f(__builtin_fmaf(yB[i], rq, nb)); l += yB[i]; yB[i] *= rkB[i]; }
  const float inv = __builtin_amdgcn_rcpf(xhalf_sum(l));
  u32x4 pf[4];
#pragma unroll
  for (int j = 0; j < 4; ++j) {
    pf[0][j] = pk2(yA[2 * j], yA[2 * j + 1]);
    pf[1][j] = pk2(yA[8 + 2 * j], yA[8 + 2 * j + 1]);
    pf[2][j] = pk2(yB[2 * j], yB[2 * j + 1]);
  }
  pf[3][0] = pk2(yB[8], 0.f); pf[3][1] = 0u; pf[3][2] = 0u; pf[3][3] = 0u;
#pragma unroll
  for (int ct = 0; ct < 3; ++ct) {
    f32x16 o = zero16();
    o = mfma32(uf[0][ct][0], pf[0], o);
    o = mfma32(uf[0][ct][1], pf[1], o);
    o = mfma32(uf[1][ct][0], pf[2], o);
    o = mfma32(uf[1][ct][1], pf[3], o);
#pragma unroll
    for (int i = 0; i < 16; ++i) {
      const unsigned q = hq[2 * ct + (i >> 3)][(i & 7) >> 1];
      o[i] = __builtin_fmaf(o[i], inv, (i & 1) ? bf_hi(q) : bf_lo(q));
    }
    __builtin_amdgcn_raw_buffer_store_b128(pack8(o, 0), orsrc, ooff + (2 * ct) * 32, 0, 0);
    __builtin_amdgcn_raw_buffer_store_b128(pack8(o, 8), orsrc, ooff + (2 * ct + 1) * 32, 0, 0);
  }
}

struct PrepA { u32x4 q[3][6], k[3][6]; };
DEV void prep_a_load(PrepA& a, const char* wqkv, int jt, int lane) {
  const int r = lane & 31, h = lane >> 5;
#pragma unroll
  for (int d = 0; d < 3; ++d) {
    int it = jt + d; it = it >= 3 ? it - 3 : it;
    const unsigned voff = (32 * it + sigma32(r)) * 576 + 16 * h;
    LD6(a.q[d], wqkv, voff, 0);
    LD6(a.k[d], wqkv, voff, 192);
  }
}
DEV void prep_a_compute(PrepA& a, LAS char* dst, LAS float* rho, LAS unsigned* cnt, int jt, int lane) {
  const int r = lane & 31, h = lane >> 5;
#pragma unroll
  for (int d = 0; d < 3; ++d) { PIN6(a.q[d]); PIN6(a.k[d]); }
  f32x16 R = zero16();
#pragma unroll
  for (int d = 0; d < 3; ++d)
#pragma unroll
    for (int s = 0; s < 6; ++s) R = mfma32(a.k[d][s], a.q[0][s], R);
  f32x16 D[3];
#pragma unroll
  for (int d = 0; d < 3; ++d) {
    D[d] = zero16();
#pragma unroll
    for (int s = 0; s < 6; ++s) D[d] = mfma32(a.q[d][s], a.k[0][s], D[d]);
  }
  float rsum = 0.f;
#pragma unroll
  for (int i = 0; i < 16; ++i) rsum += R[i];
  rsum = xhalf_sum(rsum);
  rho[32 * jt + sigma32(r)] = rsum;
  lds_arrive(cnt);
  float ks = 0.f;
#pragma unroll
  for (int d = 0; d < 3; ++d)
#pragma unroll
    for (int i = 0; i < 16; ++i) ks += D[d][i];
  ks = xhalf_sum(ks);
  lds_wait_ge(cnt, 3);
  float rv[3][16], tau = 0.f;
#pragma unroll
  for (int d = 0; d < 3; ++d) {
    int it = jt + d; it = it >= 3 ? it - 3 : it;
#pragma unroll
    for (int u = 0; u < 4; ++u) {
      const f32x4 v = *(LAS const f32x4*)(rho + 32 * it + 16 * (u >> 1) + 8 * h + 4 * (u & 1));
#pragma unroll
      for (int e = 0; e < 4; ++e) { rv[d][4 * u + e] = v[e]; tau += v[e]; }
    }
  }
  tau = xhalf_sum(tau);
  const float c0 = (tau * (1.0f / 96.0f) - ks) * (1.0f / 96.0f);
#pragma unroll
  for (int d = 0; d < 3; ++d) {
    int it = jt + d; it = it >= 3 ? it - 3 : it;
#pragma unroll
    for (int i = 0; i < 16; ++i) D[d][i] = (__builtin_fmaf(rv[d][i], -1.0f / 96.0f, D[d][i]) + c0) * kScaleLog2e;
    lds_st16(dst + (jt * 6 + 2 * it) * 1024 + lane * 16, pack8(D[d], 0));
    lds_st16(dst + (jt * 6 + 2 * it + 1) * 1024 + lane * 16, pack8(D[d], 8));
  }
}
struct PrepB { u32x4 v[3][6]; };
DEV void prep_b_load(PrepB& b, const char* wqkv, const char* wo, unsigned lds_dst, int lane) {
  const int r = lane & 31, h = lane >> 5;
#pragma unroll
  for (int i = 0; i < 18; ++i) dma16(wo, i * 1024 + lane * 16, lds_dst + i * 1024);
#pragma unroll
  for (int it = 0; it < 3; ++it) {
    const unsigned voff = (32 * it + sigma32(r)) * 576 + 16 * h;
    LD6(b.v[it], wqkv, voff, 384);
  }
}
DEV void prep_b_compute(PrepB& b, LAS char* dst, int lane) {
  const int h = lane >> 5, blk = (lane >> 4) & 1, q = (lane & 15) >> 2, p = lane & 3;
  const int psw = ((p & 1) << 1) | (p >> 1);
#pragma unroll
  for (int it = 0; it < 3; ++it) PIN6(b.v[it]);
  u32x4 o[3][6];
  LAS char* tb = dst + (8 * h + q) * 192 + (16 * blk + 4 * psw) * 2;
#pragma unroll
  for (int jt = 0; jt < 3; ++jt)
#pragma unroll
    for (int s = 0; s < 6; ++s) {
      const s16x4 lo = __builtin_amdgcn_ds_read_tr16_b64_v4i16((LAS s16x4*)(tb + (16 * s) * 192 + jt * 64));
      const s16x4 hi = __builtin_amdgcn_ds_read_tr16_b64_v4i16((LAS s16x4*)(tb + (16 * s + 4) * 192 + jt * 64));
      const u32x2 l2 = __builtin_bit_cast(u32x2, lo), h2 = __builtin_bit_cast(u32x2, hi);
      o[jt][s] = u32x4{l2[0], l2[1], h2[0], h2[1]};
    }
  asm volatile("s_waitcnt lgkmcnt(0)" ::: "memory");
#pragma unroll
  for (int jt = 0; jt < 3; ++jt) PIN6(o[jt]);
#pragma unroll
  for (int jt = 0; jt < 3; ++jt) {
    f32x16 D[3];
    float ks = 0.f;
#pragma unroll
    for (int it = 0; it < 3; ++it) {
      D[it] = zero16();
#pragma unroll
      for (int s = 0; s < 6; ++s) D[it] = mfma32(b.v[it][s], o[jt][s], D[it]);
#pragma unroll
      for (int i = 0; i < 16; ++i) ks += D[it][i];
    }
    ks = xhalf_sum(ks) * (1.0f / 96.0f);
#pragma unroll
    for (int it = 0; it < 3; ++it) {
#pragma unroll
      for (int i = 0; i < 16; ++i) D[it][i] -= ks;
      lds_st16(dst + (jt * 6 + 2 * it) * 1024 + lane * 16, pack8(D[it], 0));
      lds_st16(dst + (jt * 6 + 2 * it + 1) * 1024 + lane * 16, pack8(D[it], 8));
    }
  }
}

DEV void dma_frame(const char* x, int col, int t, int lane, unsigned lds_dst) {
  const int wh = col >> 5, ww = col & 31;
  const char* base = x + (size_t)t * FRAME_BYTES + (size_t)(wh * 7) * ROW_BYTES + ww * (7 * 192);
#pragma unroll
  for (int i = 0; i < 10; ++i) {
    const int u = i * 64 + lane;
    const int P = u / 12, f = u - P * 12;
    const int a = ((P >> 2) + 2 * t) & 3;
    const int row = P / 7, cc = P - row * 7;
    const unsigned voff = row * ROW_BYTES + cc * 192 + ((f ^ a) << 4);
    if (i < 9) dma16(base, voff, lds_dst + i * 1024);
    else dma16_12(base, voff, lds_dst + i * 1024);
  }
}

__global__ NOPK __launch_bounds__(512, 2) void swin_main(const char* __restrict__ x, char* __restrict__ out, const char* __restrict__ wqkv_t,
                                                         const char* __restrict__ wo_t, const char* __restrict__ wqkv_s, const char* __restrict__ wo_s
) {
  extern __shared__ __attribute__((aligned(16))) char smem_[];
  LAS char* smem = (LAS char*)smem_;
  const int lane = __builtin_amdgcn_workitem_id_x() & 63;
  const int w = __builtin_amdgcn_readfirstlane(__builtin_amdgcn_workitem_id_x() >> 6);
  const int g = w >> 2, t = w & 3;
  STAMP(0); RSTAMP(14);
  const __amdgpu_buffer_rsrc_t orsrc = __builtin_amdgcn_make_buffer_rsrc(out, 0, 4 * FRAME_BYTES, 0x00020000);
  const unsigned lds0 = __builtin_amdgcn_readfirstlane((unsigned)(size_t)smem);
  LAS char* xb = smem + LDS_X + g * XBUF_BYTES;
  LAS float* r2 = (LAS float*)(smem + LDS_R + g * 1024);
  LAS unsigned* cnt = (LAS unsigned*)(smem + LDS_CNT);
  const int bid = __builtin_amdgcn_workgroup_id_x();

  {
    const char* wqkv = g ? wqkv_s : wqkv_t;
    const char* wo = g ? wo_s : wo_t;
    const unsigned xdst = lds0 + LDS_X + g * XBUF_BYTES + t * (49 * 192);
    const int role = g ? (t == 0 ? 3 : t - 1) : t;
    if (role < 3) {
      PrepA a;
      prep_a_load(a, wqkv, role, lane);
      dma_frame(x, bid * 4 + g, t, lane, xdst);
      if (__builtin_amdgcn_workitem_id_x() < 16) cnt[__builtin_amdgcn_workitem_id_x()] = 0u;
      wg_barrier();
      asm volatile("s_waitcnt vmcnt(10)" ::: "memory");
      prep_a_compute(a, smem + LDS_W + (2 * g) * WMAT_BYTES, (LAS float*)(smem + LDS_RHO) + 96 * g, cnt + C_RHO_T + g, role, lane);
    } else {
      PrepB b;
      prep_b_load(b, wqkv, wo, lds0 + LDS_W + (2 * g + 1) * WMAT_BYTES, lane);
      dma_frame(x, bid * 4 + g, t, lane, xdst);
      wg_barrier();
      asm volatile("s_waitcnt vmcnt(10)" ::: "memory");
      prep_b_compute(b, smem + LDS_W + (2 * g + 1) * WMAT_BYTES, lane);
    }
    lds_arrive(cnt + C_WT + g);
  }
  STAMP(13);

#pragma unroll 1
  for (int round = 0; round < 2; ++round) {
    const int col = bid * 4 + round * 2 + g;
    if (round == 0) asm volatile("s_waitcnt vmcnt(0)" ::: "memory");
    else asm volatile("s_waitcnt vmcnt(12)" ::: "memory");
    STAMP(1 + round * 6);
    lds_arrive(cnt + g);
    lds_wait_ge(cnt + g, 8 * round + 4);
    if (round == 0) lds_wait_ge(cnt + C_WT, 4);
    STAMP(2 + round * 6);
#pragma unroll 1
    for (int tt = 0; tt < 2; ++tt) {
      const int tile = t + 4 * tt;
      if (tile < 7) t_tile(xb, r2, smem + LDS_W, smem + LDS_W + WMAT_BYTES, tile, opaque(lane));
    }
    STAMP(3 + round * 6);
    lds_arrive(cnt + g);
    lds_wait_ge(cnt + g, 8 * round + 8);
    if (round == 0) lds_wait_ge(cnt + C_WS, 4);
    STAMP(4 + round * 6);
    const int lane_s = opaque(lane);
#define lane lane_s
    const int r = lane & 31, h = lane >> 5;
    u32x4 h0[6], h1[6];
    {
      const int j0 = r, j1 = imin(32 + r, 48);
      const int a0 = ((j0 >> 2) + 2 * t) & 3, a1 = ((j1 >> 2) + 2 * t) & 3;
      LAS const char* b0 = xb + 192 * (49 * t + j0);
      LAS const char* b1 = xb + 192 * (49 * t + j1);
#pragma unroll
      for (int s = 0; s < 6; ++s) {
        h0[s] = lds_ld16(b0 + 16 * ((2 * (s & 1) + h) ^ a0) + 64 * (s >> 1));
        h1[s] = lds_ld16(b1 + 16 * ((2 * (s & 1) + h) ^ a1) + 64 * (s >> 1));
      }
    }
    LAS const float* r2t = r2 + t * 64;
    const float rq0 = r2t[r], rq1 = r2t[imin(32 + r, 48)];
    if (round == 0) {
      asm volatile("s_waitcnt lgkmcnt(0)" ::: "memory");
      asm volatile("" : "+v"(h0[0]), "+v"(h0[1]), "+v"(h0[2]), "+v"(h0[3]), "+v"(h0[4]), "+v"(h0[5]));
      asm volatile("" : "+v"(h1[0]), "+v"(h1[1]), "+v"(h1[2]), "+v"(h1[3]), "+v"(h1[4]), "+v"(h1[5]));
      dma_frame(x, col + 2, t, opaque(lane), lds0 + LDS_X + g * XBUF_BYTES + t * (49 * 192));
    }
    STAMP(5 + round * 6);
    LAS const char* wA = smem + LDS_W + 2 * WMAT_BYTES;
    LAS const char* wB = smem + LDS_W + 3 * WMAT_BYTES;
    u32x4 uf[2][3][2];
#pragma unroll
    for (int ct = 0; ct < 3; ++ct) {
      f32x16 u0 = zero16(), u1 = zero16();
#pragma unroll
      for (int s = 0; s < 6; ++s) {
        const u32x4 wv = lds_ld16(wB + (ct * 6 + s) * 1024 + lane * 16);
        u0 = mfma32(h0[s], wv, u0);
        u1 = mfma32(h1[s], wv, u1);
      }
      uf[0][ct][0] = pack8(u0, 0); uf[0][ct][1] = pack8(u0, 8);
      uf[1][ct][0] = pack8(u1, 0); uf[1][ct][1] = pack8(u1, 8);
    }
    const int wh = col >> 5, ww = col & 31;
    const unsigned obase = t * FRAME_BYTES + (wh * 7) * ROW_BYTES + ww * (7 * 192) + h * 16;
    {
      const int j = r, row = j / 7, cc = j - row * 7;
      s_qtile(h0, h0, h1, uf, wA, r2t, rq0, lane, orsrc, obase + row * ROW_BYTES + cc * 192);
    }
    {
      const int j = imin(32 + r, 48), row = j / 7, cc = j - row * 7;
      s_qtile(h1, h0, h1, uf, wA, r2t, rq1, lane, orsrc, r <= 16 ? obase + row * ROW_BYTES + cc * 192 : 0x80000000u);
    }
    STAMP(6 + round * 6); RSTAMP(15);
#undef lane
  }
}

extern "C" void kernel_launch(void* const* d_in, const int* in_sizes, int n_in, void* d_out, int out_size, void* d_ws,
                              size_t ws_size, hipStream_t stream) {
  (void)in_sizes; (void)n_in; (void)out_size; (void)ws_size; (void)d_ws;
  (void)hipFuncSetAttribute((const void*)swin_main, hipFuncAttributeMaxDynamicSharedMemorySize, LDS_BYTES);
  swin_main<<<256, 512, LDS_BYTES, stream>>>((const char*)d_in[0], (char*)d_out, (const char*)d_in[1], (const char*)d_in[2],
                                             (const char*)d_in[3], (const char*)d_in[4]
  );
}
```

```cpp
#include <hip/hip_runtime.h>
#include <hip/hip_bf16.h>
#include <stdint.h>

#if defined(__HIP_DEVICE_COMPILE__)
#define NOPK __attribute__((target("no-packed-fp32-ops")))
#else
#define NOPK
#endif
#define DEV __device__ __forceinline__ NOPK
#define LAS __attribute__((address_space(3)))

constexpr int ROW_BYTES = 224 * 96 * 2;
constexpr int FRAME_BYTES = 224 * ROW_BYTES;
constexpr int WMAT_BYTES = 18432;
constexpr int LDS_W = 0;
constexpr int LDS_X = 4 * WMAT_BYTES;
constexpr int XBUF_BYTES = 196 * 192;
constexpr int LDS_R = LDS_X + 2 * XBUF_BYTES;
constexpr int LDS_RHO = LDS_R + 2 * 1024;
constexpr int LDS_CNT = LDS_RHO + 2 * 384;
constexpr int LDS_TAB = LDS_CNT + 128;
constexpr int LDS_DUMP = LDS_TAB + 2 * 2560;
constexpr int LDS_PADW = LDS_DUMP + 256;
constexpr int LDS_PADR = LDS_PADW + 6144;
constexpr int LDS_BYTES = LDS_PADR + 192;
static_assert(LDS_BYTES <= 163840, "LDS");
enum { C_E = 0, C_L = 2, C_B3 = 4, C_WT = 6, C_WS = 7, C_TR = 8, C_QS = 9, C_QTC = 10, C_VTC = 12, C_QSC = 13, C_VSC = 15, C_M = 18 };

DEV int imin(int a, int b) { return a < b ? a : b; }
DEV int opaque(int v) { asm volatile("" : "+v"(v)); return v; }
DEV int sigma32(int r) { return (r & 19) | ((r & 4) << 1) | ((r & 8) >> 1); }
DEV const char* uptr(const void* p) {
  const unsigned long long v = (unsigned long long)p;
  const unsigned lo = __builtin_amdgcn_readfirstlane((unsigned)v), hi = __builtin_amdgcn_readfirstlane((unsigned)(v >> 32));
  return (const char*)(((unsigned long long)hi << 32) | lo);
}
DEV void lds_add(LAS unsigned* p, unsigned n) {
  const unsigned addr = (unsigned)(size_t)p;
  unsigned long long ex;
  asm volatile("s_waitcnt lgkmcnt(0)\n\ts_mov_b64 %0, exec\n\ts_mov_b64 exec, 1\n\tds_add_u32 %1, %2\n\ts_mov_b64 exec, %0"
               : "=&s"(ex) : "v"(addr), "v"(n) : "memory");
}
DEV void lds_arrive(LAS unsigned* p) { lds_add(p, 1u); }
DEV void lds_wait_ge(LAS unsigned* p, unsigned target) {
  const unsigned addr = (unsigned)(size_t)p;
#pragma nounroll
  for (int spin = 0; spin < (1 << 22); ++spin) {
    unsigned v;
    asm volatile("ds_read_b32 %0, %1\n\ts_waitcnt lgkmcnt(0)" : "=&v"(v) : "v"(addr) : "memory");
    if ((unsigned)__builtin_amdgcn_readfirstlane(v) >= target) break;
    __builtin_amdgcn_s_sleep(1);
  }
}
DEV void wg_barrier() { asm volatile("s_waitcnt lgkmcnt(0)\n\ts_barrier" ::: "memory"); }
#define VM_WAIT(n) asm volatile("s_waitcnt vmcnt(" #n ")" ::: "memory")

#define T_BLOCK_ASM \
  "s_waitcnt lgkmcnt(0)\n" \
  "ds_read_b128 v[40:43], %[ae] offset:0\n" \
  "ds_read_b128 v[44:47], %[ao] offset:0\n" \
  "ds_read_b128 v[48:51], %[ae] offset:64\n" \
  "ds_read_b128 v[52:55], %[ao] offset:64\n" \
  "ds_read_b128 v[56:59], %[ae] offset:128\n" \
  "ds_read_b128 v[60:63], %[ao] offset:128\n" \
  "ds_read_b128 v[64:67], %[wt] offset:0\n" \
  "ds_read_b128 v[68:71], %[wt] offset:1024\n" \
  "ds_read_b128 v[72:75], %[wt] offset:2048\n" \
  "ds_read_b128 v[76:79], %[wt] offset:3072\n" \
  "s_mov_b32 s64, 0xffff0000\n" \
  "s_mov_b32 s65, 0x3f803f80\n" \
  "s_mov_b32 s66, 0xf\n" \
  "s_mov_b32 s67, 0xf0\n" \
  "s_mov_b32 s68, 0xf00\n" \
  "s_mov_b32 s69, 0xf000\n" \
  "s_mov_b32 s70, 0xf0000\n" \
  "s_mov_b32 s71, 0xf00000\n" \
  "s_mov_b32 s72, 0xf000000\n" \
  "s_mov_b32 s73, 0xf0000000\n" \
  "s_waitcnt lgkmcnt(9)\n" \
  "v_lshlrev_b32_e32 v80, 16, v40\n" \
  "v_and_b32_e32 v81, s64, v40\n" \
  "v_lshlrev_b32_e32 v82, 16, v41\n" \
  "v_and_b32_e32 v83, s64, v41\n" \
  "v_mul_f32_e32 v255, v80, v80\n" \
  "v_mul_f32_e32 v254, v81, v81\n" \
  "v_lshlrev_b32_e32 v84, 16, v42\n" \
  "v_and_b32_e32 v85, s64, v42\n" \
  "s_waitcnt lgkmcnt(3)\n" \
  "v_mfma_f32_32x32x16_bf16 v[96:111], v[64:67], v[40:43], 0\n" \
  "ds_read_b128 v[64:67], %[wt] offset:4096\n" \
  "v_fmac_f32_e32 v255, v82, v82\n" \
  "v_add_f32_e32 v253, v80, v82\n" \
  "v_fmac_f32_e32 v254, v83, v83\n" \
  "v_add_f32_e32 v252, v81, v83\n" \
  "v_lshlrev_b32_e32 v86, 16, v43\n" \
  "v_and_b32_e32 v87, s64, v43\n" \
  "s_waitcnt lgkmcnt(1)\n" \
  "v_mfma_f32_32x32x16_bf16 v[96:111], v[68:71], v[44:47], v[96:111]\n" \
  "ds_read_b128 v[68:71], %[wt] offset:5120\n" \
  "v_fmac_f32_e32 v255, v84, v84\n" \
  "v_add_f32_e32 v253, v253, v84\n" \
  "v_fmac_f32_e32 v254, v85, v85\n" \
  "v_add_f32_e32 v252, v252, v85\n" \
  "v_lshlrev_b32_e32 v88, 16, v44\n" \
  "v_and_b32_e32 v89, s64, v44\n" \
  "v_mfma_f32_32x32x16_bf16 v[96:111], v[72:75], v[48:51], v[96:111]\n" \
  "ds_read_b128 v[72:75], %[wt] offset:6144\n" \
  "v_fmac_f32_e32 v255, v86, v86\n" \
  "v_add_f32_e32 v253, v253, v86\n" \
  "v_fmac_f32_e32 v254, v87, v87\n" \
  "v_add_f32_e32 v252, v252, v87\n" \
  "v_lshlrev_b32_e32 v90, 16, v45\n" \
  "v_and_b32_e32 v91, s64, v45\n" \
  "v_mfma_f32_32x32x16_bf16 v[96:111], v[76:79], v[52:55], v[96:111]\n" \
  "ds_read_b128 v[76:79], %[wt] offset:7168\n" \
  "v_fmac_f32_e32 v255, v88, v88\n" \
  "v_add_f32_e32 v253, v253, v88\n" \
  "v_fmac_f32_e32 v254, v89, v89\n" \
  "v_add_f32_e32 v252, v252, v89\n" \
  "v_lshlrev_b32_e32 v92, 16, v46\n" \
  "v_and_b32_e32 v93, s64, v46\n" \
  "s_waitcnt lgkmcnt(2)\n" \
  "v_mfma_f32_32x32x16_bf16 v[96:111], v[64:67], v[56:59], v[96:111]\n" \
  "ds_read_b128 v[64:67], %[wt] offset:8192\n" \
  "v_fmac_f32_e32 v255, v90, v90\n" \
  "v_add_f32_e32 v253, v253, v90\n" \
  "v_fmac_f32_e32 v254, v91, v91\n" \
  "v_add_f32_e32 v252, v252, v91\n" \
  "v_lshlrev_b32_e32 v94, 16, v47\n" \
  "v_and_b32_e32 v95, s64, v47\n" \
  "v_mfma_f32_32x32x16_bf16 v[96:111], v[68:71], v[60:63], v[96:111]\n" \
  "ds_read_b128 v[68:71], %[wt] offset:9216\n" \
  "v_fmac_f32_e32 v255, v92, v92\n" \
  "v_add_f32_e32 v253, v253, v92\n" \
  "v_fmac_f32_e32 v254, v93, v93\n" \
  "v_add_f32_e32 v252, v252, v93\n" \
  "v_lshlrev_b32_e32 v112, 16, v48\n" \
  "v_and_b32_e32 v113, s64, v48\n" \
  "s_waitcnt lgkmcnt(2)\n" \
  "v_mfma_f32_32x32x16_bf16 v[128:143], v[72:75], v[40:43], 0\n" \
  "ds_read_b128 v[72:75], %[wt] offset:10240\n" \
  "v_fmac_f32_e32 v255, v94, v94\n" \
  "v_add_f32_e32 v253, v253, v94\n" \
  "v_fmac_f32_e32 v254, v95, v95\n" \
  "v_add_f32_e32 v252, v252, v95\n" \
  "v_lshlrev_b32_e32 v114, 16, v49\n" \
  "v_cvt_pk_bf16_f32 v144, v96, v97\n" \
  "v_mfma_f32_32x32x16_bf16 v[128:143], v[76:79], v[44:47], v[128:143]\n" \
  "ds_read_b128 v[76:79], %[wt] offset:11264\n" \
  "v_cvt_pk_bf16_f32 v145, v98, v99\n" \
  "v_cvt_pk_bf16_f32 v146, v100, v101\n" \
  "v_cvt_pk_bf16_f32 v147, v102, v103\n" \
  "v_cvt_pk_bf16_f32 v148, v104, v105\n" \
  "v_cvt_pk_bf16_f32 v149, v106, v107\n" \
  "s_waitcnt lgkmcnt(2)\n" \
  "v_mfma_f32_32x32x16_bf16 v[128:143], v[64:67], v[48:51], v[128:143]\n" \
  "ds_read_b128 v[64:67], %[wt] offset:12288\n" \
  "v_cvt_pk_bf16_f32 v150, v108, v109\n" \
  "v_cvt_pk_bf16_f32 v151, v110, v111\n" \
  "v_and_b32_e32 v115, s64, v49\n" \
  "v_fmac_f32_e32 v255, v112, v112\n" \
  "v_add_f32_e32 v253, v253, v112\n" \
  "v_fmac_f32_e32 v254, v113, v113\n" \
  "v_mfma_f32_32x32x16_bf16 v[128:143], v[68:71], v[52:55], v[128:143]\n" \
  "ds_read_b128 v[68:71], %[wt] offset:13312\n" \
  "v_add_f32_e32 v252, v252, v113\n" \
  "v_lshlrev_b32_e32 v116, 16, v50\n" \
  "v_and_b32_e32 v117, s64, v50\n" \
  "v_fmac_f32_e32 v255, v114, v114\n" \
  "v_add_f32_e32 v253, v253, v114\n" \
  "v_fmac_f32_e32 v254, v115, v115\n" \
  "s_waitcnt lgkmcnt(2)\n" \
  "v_mfma_f32_32x32x16_bf16 v[128:143], v[72:75], v[56:59], v[128:143]\n" \
  "ds_read_b128 v[72:75], %[wt] offset:14336\n" \
  "v_add_f32_e32 v252, v252, v115\n" \
  "v_lshlrev_b32_e32 v118, 16, v51\n" \
  "v_and_b32_e32 v119, s64, v51\n" \
  "v_fmac_f32_e32 v255, v116, v116\n" \
  "v_add_f32_e32 v253, v253, v116\n" \
  "v_fmac_f32_e32 v254, v117, v117\n" \
  "v_mfma_f32_32x32x16_bf16 v[128:143], v[76:79], v[60:63], v[128:143]\n" \
  "ds_read_b128 v[76:79], %[wt] offset:15360\n" \
  "v_add_f32_e32 v252, v252, v117\n" \
  "v_lshlrev_b32_e32 v120, 16, v52\n" \
  "v_and_b32_e32 v121, s64, v52\n" \
  "v_fmac_f32_e32 v255, v118, v118\n" \
  "v_add_f32_e32 v253, v253, v118\n" \
  "v_fmac_f32_e32 v254, v119, v119\n" \
  "s_waitcnt lgkmcnt(2)\n" \
  "v_mfma_f32_32x32x16_bf16 v[96:111], v[64:67], v[40:43], 0\n" \
  "ds_read_b128 v[64:67], %[wt] offset:16384\n" \
  "v_add_f32_e32 v252, v252, v119\n" \
  "v_lshlrev_b32_e32 v122, 16, v53\n" \
  "v_and_b32_e32 v123, s64, v53\n" \
  "v_fmac_f32_e32 v255, v120, v120\n" \
  "v_add_f32_e32 v253, v253, v120\n" \
  "v_cvt_pk_bf16_f32 v152, v128, v129\n" \
  "v_mfma_f32_32x32x16_bf16 v[96:111], v[68:71], v[44:47], v[96:111]\n" \
  "ds_read_b128 v[68:71], %[wt] offset:17408\n" \
  "v_cvt_pk_bf16_f32 v153, v130, v131\n" \
  "v_cvt_pk_bf16_f32 v154, v132, v133\n" \
  "v_cvt_pk_bf16_f32 v155, v134, v135\n" \
  "v_cvt_pk_bf16_f32 v156, v136, v137\n" \
  "v_cvt_pk_bf16_f32 v157, v138, v139\n" \
  "s_waitcnt lgkmcnt(2)\n" \
  "v_mfma_f32_32x32x16_bf16 v[96:111], v[72:75], v[48:51], v[96:111]\n" \
  "ds_read_b128 v[72:75], %[wt] offset:18432\n" \
  "v_fmac_f32_e32 v254, v121, v121\n" \
  "v_add_f32_e32 v252, v252, v121\n" \
  "v_lshlrev_b32_e32 v124, 16, v54\n" \
  "v_and_b32_e32 v125, s64, v54\n" \
  "v_fmac_f32_e32 v255, v122, v122\n" \
  "v_add_f32_e32 v253, v253, v122\n" \
  "v_mfma_f32_32x32x16_bf16 v[96:111], v[76:79], v[52:55], v[96:111]\n" \
  "ds_read_b128 v[76:79], %[wt] offset:19456\n" \
  "v_fmac_f32_e32 v254, v123, v123\n" \
  "v_add_f32_e32 v252, v252, v123\n" \
  "v_cvt_pk_bf16_f32 v158, v140, v141\n" \
  "v_cvt_pk_bf16_f32 v159, v142, v143\n" \
  "v_lshlrev_b32_e32 v126, 16, v55\n" \
  "v_and_b32_e32 v127, s64, v55\n" \
  "s_waitcnt lgkmcnt(2)\n" \
  "v_mfma_f32_32x32x16_bf16 v[96:111], v[64:67], v[56:59], v[96:111]\n" \
  "ds_read_b128 v[64:67], %[wt] offset:20480\n" \
  "v_fmac_f32_e32 v255, v124, v124\n" \
  "v_add_f32_e32 v253, v253, v124\n" \
  "v_fmac_f32_e32 v254, v125, v125\n" \
  "v_add_f32_e32 v252, v252, v125\n" \
  "v_lshlrev_b32_e32 v128, 16, v56\n" \
  "v_and_b32_e32 v129, s64, v56\n" \
  "v_mfma_f32_32x32x16_bf16 v[96:111], v[68:71], v[60:63], v[96:111]\n" \
  "ds_read_b128 v[68:71], %[wt] offset:21504\n" \
  "v_fmac_f32_e32 v255, v126, v126\n" \
  "v_add_f32_e32 v253, v253, v126\n" \
  "v_fmac_f32_e32 v254, v127, v127\n" \
  "v_add_f32_e32 v252, v252, v127\n" \
  "v_lshlrev_b32_e32 v130, 16, v57\n" \
  "v_and_b32_e32 v131, s64, v57\n" \
  "v_mfma_f32_4x4x4_16b_bf16 v[160:163], v[40:41], v[144:145], 0\n" \
  "v_lshlrev_b32_e32 v132, 16, v58\n" \
  "s_nop 0\n" \
  "v_mfma_f32_4x4x4_16b_bf16 v[160:163], v[42:43], v[146:147], v[160:163]\n" \
  "v_and_b32_e32 v133, s64, v58\n" \
  "s_nop 0\n" \
  "v_mfma_f32_4x4x4_16b_bf16 v[160:163], v[44:45], v[148:149], v[160:163]\n" \
  "v_lshlrev_b32_e32 v134, 16, v59\n" \
  "s_nop 0\n" \
  "v_mfma_f32_4x4x4_16b_bf16 v[160:163], v[46:47], v[150:151], v[160:163]\n" \
  "v_and_b32_e32 v135, s64, v59\n" \
  "s_nop 0\n" \
  "v_mfma_f32_4x4x4_16b_bf16 v[160:163], v[48:49], v[152:153], v[160:163]\n" \
  "v_lshlrev_b32_e32 v136, 16, v60\n" \
  "s_nop 0\n" \
  "v_mfma_f32_4x4x4_16b_bf16 v[160:163], v[50:51], v[154:155], v[160:163]\n" \
  "v_and_b32_e32 v137, s64, v60\n" \
  "s_nop 0\n" \
  "v_mfma_f32_4x4x4_16b_bf16 v[160:163], v[52:53], v[156:157], v[160:163]\n" \
  "v_lshlrev_b32_e32 v138, 16, v61\n" \
  "s_nop 0\n" \
  "v_mfma_f32_4x4x4_16b_bf16 v[160:163], v[54:55], v[158:159], v[160:163]\n" \
  "v_and_b32_e32 v139, s64, v61\n" \
  "s_waitcnt lgkmcnt(0)\n" \
  "v_mfma_f32_32x32x16_bf16 v[144:159], v[40:43], v[72:75], 0\n" \
  "ds_read_b128 v[72:75], %[wt] offset:22528\n" \
  "v_fmac_f32_e32 v255, v128, v128\n" \
  "v_add_f32_e32 v253, v253, v128\n" \
  "v_fmac_f32_e32 v254, v129, v129\n" \
  "v_add_f32_e32 v252, v252, v129\n" \
  "v_cvt_pk_bf16_f32 v164, v96, v97\n" \
  "v_cvt_pk_bf16_f32 v165, v98, v99\n" \
  "v_mfma_f32_32x32x16_bf16 v[144:159], v[44:47], v[76:79], v[144:159]\n" \
  "ds_read_b128 v[76:79], %[wt] offset:23552\n" \
  "v_fmac_f32_e32 v255, v130, v130\n" \
  "v_add_f32_e32 v253, v253, v130\n" \
  "v_fmac_f32_e32 v254, v131, v131\n" \
  "v_add_f32_e32 v252, v252, v131\n" \
  "v_fmac_f32_e32 v255, v132, v132\n" \
  "v_add_f32_e32 v253, v253, v132\n" \
  "v_mfma_f32_4x4x4_16b_bf16 v[160:163], v[56:57], v[164:165], v[160:163]\n" \
  "v_lshlrev_b32_e32 v140, 16, v62\n" \
  "v_mfma_f32_32x32x16_bf16 v[144:159], v[48:51], v[64:67], v[144:159]\n" \
  "ds_read_b128 v[64:67], %[wt] offset:24576\n" \
  "v_fmac_f32_e32 v254, v133, v133\n" \
  "v_add_f32_e32 v252, v252, v133\n" \
  "v_cvt_pk_bf16_f32 v166, v100, v101\n" \
  "v_cvt_pk_bf16_f32 v167, v102, v103\n" \
  "v_fmac_f32_e32 v255, v134, v134\n" \
  "v_add_f32_e32 v253, v253, v134\n" \
  "v_mfma_f32_4x4x4_16b_bf16 v[160:163], v[58:59], v[166:167], v[160:163]\n" \
  "v_and_b32_e32 v141, s64, v62\n" \
  "v_mfma_f32_32x32x16_bf16 v[144:159], v[52:55], v[68:71], v[144:159]\n" \
  "ds_read_b128 v[68:71], %[wt] offset:25600\n" \
  "v_fmac_f32_e32 v254, v135, v135\n" \
  "v_add_f32_e32 v252, v252, v135\n" \
  "v_fmac_f32_e32 v255, v136, v136\n" \
  "v_add_f32_e32 v253, v253, v136\n" \
  "v_fmac_f32_e32 v254, v137, v137\n" \
  "v_add_f32_e32 v252, v252, v137\n" \
  "s_waitcnt lgkmcnt(2)\n" \
  "v_mfma_f32_32x32x16_bf16 v[144:159], v[56:59], v[72:75], v[144:159]\n" \
  "ds_read_b128 v[72:75], %[wt] offset:26624\n" \
  "v_fmac_f32_e32 v255, v138, v138\n" \
  "v_add_f32_e32 v253, v253, v138\n" \
  "v_fmac_f32_e32 v254, v139, v139\n" \
  "v_add_f32_e32 v252, v252, v139\n" \
  "v_cvt_pk_bf16_f32 v164, v104, v105\n" \
  "v_cvt_pk_bf16_f32 v165, v106, v107\n" \
  "v_mfma_f32_32x32x16_bf16 v[144:159], v[60:63], v[76:79], v[144:159]\n" \
  "ds_read_b128 v[76:79], %[wt] offset:27648\n" \
  "v_lshlrev_b32_e32 v142, 16, v63\n" \
  "v_and_b32_e32 v143, s64, v63\n" \
  "v_fmac_f32_e32 v255, v140, v140\n" \
  "v_add_f32_e32 v253, v253, v140\n" \
  "v_fmac_f32_e32 v254, v141, v141\n" \
  "v_add_f32_e32 v252, v252, v141\n" \
  "v_mfma_f32_4x4x4_16b_bf16 v[160:163], v[60:61], v[164:165], v[160:163]\n" \
  "v_fmac_f32_e32 v255, v142, v142\n" \
  "s_waitcnt lgkmcnt(2)\n" \
  "v_mfma_f32_32x32x16_bf16 v[168:183], v[40:43], v[64:67], 0\n" \
  "ds_read_b128 v[64:67], %[wt] offset:28672\n" \
  "v_add_f32_e32 v253, v253, v142\n" \
  "v_fmac_f32_e32 v254, v143, v143\n" \
  "v_add_f32_e32 v252, v252, v143\n" \
  "v_add_f32_e32 v255, v255, v254\n" \
  "v_add_f32_e32 v253, v253, v252\n" \
  "v_cvt_pk_bf16_f32 v166, v108, v109\n" \
  "v_mfma_f32_32x32x16_bf16 v[168:183], v[44:47], v[68:71], v[168:183]\n" \
  "ds_read_b128 v[68:71], %[wt] offset:29696\n" \
  "v_cvt_pk_bf16_f32 v167, v110, v111\n" \
  "v_permlane32_swap_b32_e32 v253, v255\n" \
  "v_cvt_pk_bf16_f32 v96, v144, v145\n" \
  "v_add_f32_e32 v253, v253, v255\n" \
  "v_mov_b32_e32 v255, v253\n" \
  "v_mfma_f32_4x4x4_16b_bf16 v[160:163], v[62:63], v[166:167], v[160:163]\n" \
  "s_nop 0\n" \
  "v_permlane32_swap_b32_e32 v253, v255\n" \
  "v_cvt_pk_bf16_f32 v97, v146, v147\n" \
  "s_waitcnt lgkmcnt(2)\n" \
  "v_mfma_f32_32x32x16_bf16 v[168:183], v[48:51], v[72:75], v[168:183]\n" \
  "ds_read_b128 v[72:75], %[wt] offset:30720\n" \
  "v_permlane32_swap_b32_e32 v160, v161\n" \
  "v_mul_f32_e32 v253, 0x3c2aaaab, v253\n" \
  "v_permlane32_swap_b32_e32 v162, v163\n" \
  "v_mul_f32_e32 v255, 0x3c2aaaab, v255\n" \
  "v_mfma_f32_32x32x16_bf16 v[168:183], v[52:55], v[76:79], v[168:183]\n" \
  "ds_read_b128 v[76:79], %[wt] offset:31744\n" \
  "v_add_f32_e32 v160, v160, v161\n" \
  "v_add_f32_e32 v162, v162, v163\n" \
  "v_mov_b32_e32 v161, v160\n" \
  "v_mov_b32_e32 v163, v162\n" \
  "v_fma_f32 v255, -v253, v253, v255\n" \
  "v_max_f32_e32 v255, 0, v255\n" \
  "s_waitcnt lgkmcnt(2)\n" \
  "v_mfma_f32_32x32x16_bf16 v[168:183], v[56:59], v[64:67], v[168:183]\n" \
  "ds_read_b128 v[64:67], %[wt] offset:32768\n" \
  "v_add_f32_e32 v255, 0x3727c5ac, v255\n" \
  "v_permlane32_swap_b32_e32 v160, v161\n" \
  "v_rsq_f32_e32 v254, v255\n" \
  "v_permlane32_swap_b32_e32 v162, v163\n" \
  "s_nop 0\n" \
  "v_mul_f32_dpp v160, v254, v160 quad_perm:[0,0,0,0] row_mask:0xf bank_mask:0xf\n" \
  "v_mfma_f32_32x32x16_bf16 v[168:183], v[60:63], v[68:71], v[168:183]\n" \
  "ds_read_b128 v[68:71], %[wt] offset:33792\n" \
  "v_mul_f32_dpp v161, v254, v161 quad_perm:[1,1,1,1] row_mask:0xf bank_mask:0xf\n" \
  "v_mul_f32_dpp v162, v254, v162 quad_perm:[2,2,2,2] row_mask:0xf bank_mask:0xf\n" \
  "v_mul_f32_dpp v163, v254, v163 quad_perm:[3,3,3,3] row_mask:0xf bank_mask:0xf\n" \
  "v_max3_f32 v255, v160, v161, v162\n" \
  "v_max_f32_e32 v255, v255, v163\n" \
  "s_waitcnt lgkmcnt(2)\n" \
  "v_mfma_f32_32x32x16_bf16 v[184:199], v[40:43], v[72:75], 0\n" \
  "ds_read_b128 v[40:43], %[wt] offset:34816\n" \
  "v_mul_f32_e64 v253, -v255, v254\n" \
  "v_fma_f32 v160, v160, v254, v253\n" \
  "v_fma_f32 v161, v161, v254, v253\n" \
  "v_fma_f32 v162, v162, v254, v253\n" \
  "v_fma_f32 v163, v163, v254, v253\n" \
  "v_mfma_f32_32x32x16_bf16 v[184:199], v[44:47], v[76:79], v[184:199]\n" \
  "ds_read_b128 v[44:47], %[wt] offset:35840\n" \
  "v_exp_f32_e32 v160, v160\n" \
  "v_exp_f32_e32 v161, v161\n" \
  "v_exp_f32_e32 v162, v162\n" \
  "v_exp_f32_e32 v163, v163\n" \
  "v_add_f32_e32 v255, v160, v161\n" \
  "s_waitcnt lgkmcnt(2)\n" \
  "v_mfma_f32_32x32x16_bf16 v[184:199], v[48:51], v[64:67], v[184:199]\n" \
  "v_add_f32_e32 v253, v162, v163\n" \
  "v_add_f32_e32 v255, v255, v253\n" \
  "v_rcp_f32_e32 v253, v255\n" \
  "v_cvt_pk_bf16_f32 v98, v148, v149\n" \
  "s_nop 0\n" \
  "v_mul_f32_dpp v48, v254, v253 quad_perm:[0,0,0,0] row_mask:0xf bank_mask:0xf\n" \
  "v_mfma_f32_32x32x16_bf16 v[184:199], v[52:55], v[68:71], v[184:199]\n" \
  "v_mul_f32_dpp v49, v254, v253 quad_perm:[1,1,1,1] row_mask:0xf bank_mask:0xf\n" \
  "v_mul_f32_dpp v50, v254, v253 quad_perm:[2,2,2,2] row_mask:0xf bank_mask:0xf\n" \
  "v_mul_f32_dpp v51, v254, v253 quad_perm:[3,3,3,3] row_mask:0xf bank_mask:0xf\n" \
  "v_mul_f32_e32 v160, v48, v160\n" \
  "v_mul_f32_e32 v161, v49, v161\n" \
  "v_mul_f32_e32 v162, v50, v162\n" \
  "s_waitcnt lgkmcnt(0)\n" \
  "v_mfma_f32_32x32x16_bf16 v[184:199], v[56:59], v[40:43], v[184:199]\n" \
  "v_mul_f32_e32 v163, v51, v163\n" \
  "v_cvt_pk_bf16_f32 v255, v160, v161\n" \
  "s_waitcnt vmcnt(4)\n" \
  "s_mov_b64 s[74:75], exec\n" \
  "s_mov_b64 exec, 1\n" \
  "ds_add_u32 %[cnt], %[flag] offset:0\n" \
  "s_mov_b64 exec, s[74:75]\n" \
  "v_cvt_pk_bf16_f32 v254, v162, v163\n" \
  "v_cndmask_b32_e64 v40, 0, v255, s[66:67]\n" \
  "v_mfma_f32_32x32x16_bf16 v[184:199], v[60:63], v[44:47], v[184:199]\n" \
  "v_cndmask_b32_e64 v41, 0, v254, s[66:67]\n" \
  "v_cndmask_b32_e64 v42, 0, v255, s[68:69]\n" \
  "v_cndmask_b32_e64 v43, 0, v254, s[68:69]\n" \
  "v_cvt_pk_bf16_f32 v99, v150, v151\n" \
  "v_cndmask_b32_e64 v44, 0, v255, s[70:71]\n" \
  "v_cndmask_b32_e64 v45, 0, v254, s[70:71]\n" \
  "v_mfma_f32_32x32x16_bf16 v[80:95], v[96:99], v[40:43], v[80:95]\n" \
  "v_cndmask_b32_e64 v46, 0, v255, s[72:73]\n" \
  "v_cndmask_b32_e64 v47, 0, v254, s[72:73]\n" \
  "v_cvt_pk_bf16_f32 v48, v152, v153\n" \
  "v_cvt_pk_bf16_f32 v49, v154, v155\n" \
  "v_cvt_pk_bf16_f32 v50, v156, v157\n" \
  "v_cvt_pk_bf16_f32 v51, v158, v159\n" \
  "v_cvt_pk_bf16_f32 v52, v168, v169\n" \
  "v_cvt_pk_bf16_f32 v53, v170, v171\n" \
  "v_mfma_f32_32x32x16_bf16 v[80:95], v[48:51], v[44:47], v[80:95]\n" \
  "v_cvt_pk_bf16_f32 v54, v172, v173\n" \
  "v_cvt_pk_bf16_f32 v55, v174, v175\n" \
  "v_cvt_pk_bf16_f32 v48, v176, v177\n" \
  "v_cvt_pk_bf16_f32 v49, v178, v179\n" \
  "v_cvt_pk_bf16_f32 v50, v180, v181\n" \
  "v_mfma_f32_32x32x16_bf16 v[112:127], v[52:55], v[40:43], v[112:127]\n" \
  "v_cvt_pk_bf16_f32 v51, v182, v183\n" \
  "v_cvt_pk_bf16_f32 v52, v184, v185\n" \
  "v_cvt_pk_bf16_f32 v53, v186, v187\n" \
  "v_cvt_pk_bf16_f32 v54, v188, v189\n" \
  "s_nop 1\n" \
  "v_cvt_pk_bf16_f32 v56, v80, v81\n" \
  "v_mfma_f32_32x32x16_bf16 v[112:127], v[48:51], v[44:47], v[112:127]\n" \
  "v_cvt_pk_bf16_f32 v55, v190, v191\n" \
  "v_cvt_pk_bf16_f32 v57, v82, v83\n" \
  "v_cvt_pk_bf16_f32 v58, v84, v85\n" \
  "v_cvt_pk_bf16_f32 v59, v86, v87\n" \
  "ds_write_b128 %[ae], v[56:59] offset:0\n" \
  "v_mfma_f32_32x32x16_bf16 v[128:143], v[52:55], v[40:43], v[128:143]\n" \
  "v_cvt_pk_bf16_f32 v40, v192, v193\n" \
  "v_cvt_pk_bf16_f32 v48, v88, v89\n" \
  "v_cvt_pk_bf16_f32 v49, v90, v91\n" \
  "v_cvt_pk_bf16_f32 v50, v92, v93\n" \
  "v_cvt_pk_bf16_f32 v51, v94, v95\n" \
  "ds_write_b128 %[ao], v[48:51] offset:0\n" \
  "v_dot2_f32_bf16 v255, v56, v56, 0\n" \
  "v_dot2_f32_bf16 v254, s65, v56, 0\n" \
  "v_dot2_f32_bf16 v253, v57, v57, 0\n" \
  "v_dot2_f32_bf16 v252, s65, v57, 0\n" \
  "v_dot2c_f32_bf16_e32 v255, v58, v58\n" \
  "v_dot2c_f32_bf16_e32 v254, s65, v58\n" \
  "v_dot2c_f32_bf16_e32 v253, v59, v59\n" \
  "v_dot2c_f32_bf16_e32 v252, s65, v59\n" \
  "v_dot2c_f32_bf16_e32 v255, v48, v48\n" \
  "v_dot2c_f32_bf16_e32 v254, s65, v48\n" \
  "v_dot2c_f32_bf16_e32 v253, v49, v49\n" \
  "v_dot2c_f32_bf16_e32 v252, s65, v49\n" \
  "v_dot2c_f32_bf16_e32 v255, v50, v50\n" \
  "v_dot2c_f32_bf16_e32 v254, s65, v50\n" \
  "v_dot2c_f32_bf16_e32 v253, v51, v51\n" \
  "v_dot2c_f32_bf16_e32 v252, s65, v51\n" \
  "v_cvt_pk_bf16_f32 v48, v112, v113\n" \
  "v_dot2c_f32_bf16_e32 v255, v48, v48\n" \
  "v_dot2c_f32_bf16_e32 v254, s65, v48\n" \
  "v_cvt_pk_bf16_f32 v41, v194, v195\n" \
  "v_cvt_pk_bf16_f32 v42, v196, v197\n" \
  "v_cvt_pk_bf16_f32 v43, v198, v199\n" \
  "v_cvt_pk_bf16_f32 v49, v114, v115\n" \
  "v_dot2c_f32_bf16_e32 v253, v49, v49\n" \
  "v_mfma_f32_32x32x16_bf16 v[128:143], v[40:43], v[44:47], v[128:143]\n" \
  "v_cvt_pk_bf16_f32 v50, v116, v117\n" \
  "v_cvt_pk_bf16_f32 v51, v118, v119\n" \
  "ds_write_b128 %[ae], v[48:51] offset:64\n" \
  "v_cvt_pk_bf16_f32 v40, v120, v121\n" \
  "v_cvt_pk_bf16_f32 v41, v122, v123\n" \
  "v_dot2c_f32_bf16_e32 v252, s65, v49\n" \
  "v_dot2c_f32_bf16_e32 v255, v50, v50\n" \
  "v_dot2c_f32_bf16_e32 v254, s65, v50\n" \
  "v_dot2c_f32_bf16_e32 v253, v51, v51\n" \
  "v_dot2c_f32_bf16_e32 v252, s65, v51\n" \
  "v_dot2c_f32_bf16_e32 v255, v40, v40\n" \
  "v_dot2c_f32_bf16_e32 v254, s65, v40\n" \
  "v_dot2c_f32_bf16_e32 v253, v41, v41\n" \
  "v_dot2c_f32_bf16_e32 v252, s65, v41\n" \
  "v_cvt_pk_bf16_f32 v42, v124, v125\n" \
  "v_dot2c_f32_bf16_e32 v255, v42, v42\n" \
  "v_dot2c_f32_bf16_e32 v254, s65, v42\n" \
  "v_cvt_pk_bf16_f32 v43, v126, v127\n" \
  "ds_write_b128 %[ao], v[40:43] offset:64\n" \
  "v_dot2c_f32_bf16_e32 v253, v43, v43\n" \
  "v_dot2c_f32_bf16_e32 v252, s65, v43\n" \
  "v_cvt_pk_bf16_f32 v40, v128, v129\n" \
  "v_dot2c_f32_bf16_e32 v255, v40, v40\n" \
  "v_dot2c_f32_bf16_e32 v254, s65, v40\n" \
  "v_cvt_pk_bf16_f32 v41, v130, v131\n" \
  "v_dot2c_f32_bf16_e32 v253, v41, v41\n" \
  "v_dot2c_f32_bf16_e32 v252, s65, v41\n" \
  "v_cvt_pk_bf16_f32 v42, v132, v133\n" \
  "v_dot2c_f32_bf16_e32 v255, v42, v42\n" \
  "v_dot2c_f32_bf16_e32 v254, s65, v42\n" \
  "v_cvt_pk_bf16_f32 v43, v134, v135\n" \
  "ds_write_b128 %[ae], v[40:43] offset:128\n" \
  "v_dot2c_f32_bf16_e32 v253, v43, v43\n" \
  "v_dot2c_f32_bf16_e32 v252, s65, v43\n" \
  "v_cvt_pk_bf16_f32 v40, v136, v137\n" \
  "v_dot2c_f32_bf16_e32 v255, v40, v40\n" \
  "v_dot2c_f32_bf16_e32 v254, s65, v40\n" \
  "v_cvt_pk_bf16_f32 v41, v138, v139\n" \
  "v_dot2c_f32_bf16_e32 v253, v41, v41\n" \
  "v_dot2c_f32_bf16_e32 v252, s65, v41\n" \
  "v_cvt_pk_bf16_f32 v42, v140, v141\n" \
  "v_dot2c_f32_bf16_e32 v255, v42, v42\n" \
  "v_dot2c_f32_bf16_e32 v254, s65, v42\n" \
  "v_cvt_pk_bf16_f32 v43, v142, v143\n" \
  "ds_write_b128 %[ao], v[40:43] offset:128\n" \
  "v_dot2c_f32_bf16_e32 v253, v43, v43\n" \
  "v_dot2c_f32_bf16_e32 v252, s65, v43\n" \
  "s_nop 1\n" \
  "v_add_f32_e32 v255, v255, v253\n" \
  "v_add_f32_e32 v254, v254, v252\n" \
  "s_nop 1\n" \
  "v_permlane32_swap_b32_e32 v254, v255\n" \
  "v_add_f32_e32 v254, v254, v255\n" \
  "v_mov_b32_e32 v255, v254\n" \
  "s_nop 1\n" \
  "v_permlane32_swap_b32_e32 v254, v255\n" \
  "v_mul_f32_e32 v254, 0x3c2aaaab, v254\n" \
  "v_mul_f32_e32 v255, 0x3c2aaaab, v255\n" \
  "v_fma_f32 v255, -v254, v254, v255\n" \
  "v_max_f32_e32 v255, 0, v255\n" \
  "v_add_f32_e32 v255, 0x3727c5ac, v255\n" \
  "v_rsq_f32_e32 v254, v255\n" \
  "ds_write_b32 %[r2a], v254 offset:0\n"

#define S_BLOCK_ASM \
  "s_waitcnt lgkmcnt(0)\n" \
  "ds_read_b128 v[24:27], %[h0e] offset:0\n" \
  "ds_read_b128 v[28:31], %[h0o] offset:0\n" \
  "ds_read_b128 v[32:35], %[h0e] offset:64\n" \
  "ds_read_b128 v[36:39], %[h0o] offset:64\n" \
  "ds_read_b128 v[40:43], %[h0e] offset:128\n" \
  "ds_read_b128 v[44:47], %[h0o] offset:128\n" \
  "ds_read_b128 v[48:51], %[h1e] offset:6144\n" \
  "ds_read_b128 v[52:55], %[h1o] offset:6144\n" \
  "ds_read_b128 v[56:59], %[h1e] offset:6208\n" \
  "ds_read_b128 v[60:63], %[h1o] offset:6208\n" \
  "ds_read_b128 v[64:67], %[h1e] offset:6272\n" \
  "ds_read_b128 v[68:71], %[h1o] offset:6272\n" \
  "ds_read_b128 v[72:75], %[ws] offset:0\n" \
  "ds_read_b128 v[76:79], %[ws] offset:1024\n" \
  "ds_read_b128 v[80:83], %[ws] offset:2048\n" \
  "s_waitcnt lgkmcnt(13)\n" \
  "ds_read_b128 v[84:87], %[ws] offset:3072\n" \
  "ds_read_b32 v255, %[rq0a] offset:0\n" \
  "s_mov_b32 s64, 0xffff0000\n" \
  "s_mov_b32 s66, 0\n" \
  "s_mov_b32 s67, -1\n" \
  "s_mov_b32 s70, m0\n" \
  "s_add_u32 s68, %[xdst], 4096\n" \
  "s_add_u32 s69, %[xdst], 8192\n" \
  "s_cmp_eq_u32 %[round], 0\n" \
  "s_waitcnt lgkmcnt(13)\n" \
  "ds_read_b32 v254, %[rq1a] offset:0\n" \
  "ds_read_b32 v253, %[tab] offset:1536\n" \
  "s_waitcnt lgkmcnt(13)\n" \
  "ds_read_b32 v252, %[tab] offset:1792\n" \
  "ds_read_b32 v251, %[tab] offset:2048\n" \
  "s_waitcnt lgkmcnt(13)\n" \
  "ds_read_b32 v250, %[tab] offset:2304\n" \
  "s_waitcnt lgkmcnt(9)\n" \
  "v_mfma_f32_32x32x16_bf16 v[88:103], v[72:75], v[24:27], 0\n" \
  "ds_read_b128 v[72:75], %[ws] offset:4096\n" \
  "s_waitcnt lgkmcnt(8)\n" \
  "v_mfma_f32_32x32x16_bf16 v[88:103], v[76:79], v[28:31], v[88:103]\n" \
  "ds_read_b128 v[76:79], %[ws] offset:5120\n" \
  "s_waitcnt lgkmcnt(5)\n" \
  "s_mov_b32 m0, s68\n" \
  "s_cbranch_scc0 Ldma6_%=\n" \
  "global_load_lds_dwordx4 v253, %[xsrc] offset:2048\n" \
  "Ldma6_%=:\n" \
  "v_mfma_f32_32x32x16_bf16 v[88:103], v[80:83], v[32:35], v[88:103]\n" \
  "s_waitcnt lgkmcnt(3)\n" \
  "s_mov_b32 m0, s68\n" \
  "s_cbranch_scc0 Ldma7_%=\n" \
  "global_load_lds_dwordx4 v252, %[xsrc] offset:3072\n" \
  "Ldma7_%=:\n" \
  "s_mov_b32 m0, s69\n" \
  "s_cbranch_scc0 Ldma8_%=\n" \
  "global_load_lds_dwordx4 v251, %[xsrc] offset:0\n" \
  "Ldma8_%=:\n" \
  "v_mfma_f32_32x32x16_bf16 v[88:103], v[84:87], v[36:39], v[88:103]\n" \
  "s_waitcnt lgkmcnt(1)\n" \
  "s_mov_b32 m0, s69\n" \
  "s_cbranch_scc0 Ldma9_%=\n" \
  "s_mov_b64 s[72:73], exec\n" \
  "s_mov_b64 exec, 0xfff\n" \
  "global_load_lds_dwordx4 v250, %[xsrc] offset:1024\n" \
  "s_mov_b64 exec, s[72:73]\n" \
  "Ldma9_%=:\n" \
  "ds_read_b128 v[80:83], %[ws] offset:6144\n" \
  "v_mfma_f32_32x32x16_bf16 v[88:103], v[72:75], v[40:43], v[88:103]\n" \
  "ds_read_b128 v[72:75], %[ws] offset:7168\n" \
  "ds_read_b128 v[84:87], %[ws] offset:8192\n" \
  "s_waitcnt lgkmcnt(3)\n" \
  "v_mfma_f32_32x32x16_bf16 v[88:103], v[76:79], v[44:47], v[88:103]\n" \
  "ds_read_b128 v[76:79], %[ws] offset:9216\n" \
  "s_waitcnt lgkmcnt(3)\n" \
  "v_mfma_f32_32x32x16_bf16 v[104:119], v[80:83], v[24:27], 0\n" \
  "ds_read_b128 v[80:83], %[ws] offset:10240\n" \
  "s_nop 7\n" \
  "v_cvt_pk_bf16_f32 v120, v88, v89\n" \
  "v_cvt_pk_bf16_f32 v121, v90, v91\n" \
  "s_waitcnt lgkmcnt(2)\n" \
  "v_mfma_f32_32x32x16_bf16 v[104:119], v[72:75], v[28:31], v[104:119]\n" \
  "ds_read_b128 v[72:75], %[ws] offset:11264\n" \
  "v_cvt_pk_bf16_f32 v122, v92, v93\n" \
  "v_cvt_pk_bf16_f32 v123, v94, v95\n" \
  "v_cvt_pk_bf16_f32 v124, v96, v97\n" \
  "v_cvt_pk_bf16_f32 v125, v98, v99\n" \
  "v_cvt_pk_bf16_f32 v126, v100, v101\n" \
  "v_mfma_f32_32x32x16_bf16 v[104:119], v[84:87], v[32:35], v[104:119]\n" \
  "ds_read_b128 v[84:87], %[ws] offset:12288\n" \
  "v_cvt_pk_bf16_f32 v127, v102, v103\n" \
  "s_waitcnt lgkmcnt(2)\n" \
  "v_mfma_f32_32x32x16_bf16 v[104:119], v[76:79], v[36:39], v[104:119]\n" \
  "ds_read_b128 v[76:79], %[ws] offset:13312\n" \
  "v_mfma_f32_32x32x16_bf16 v[104:119], v[80:83], v[40:43], v[104:119]\n" \
  "ds_read_b128 v[80:83], %[ws] offset:14336\n" \
  "s_waitcnt lgkmcnt(2)\n" \
  "v_mfma_f32_32x32x16_bf16 v[104:119], v[72:75], v[44:47], v[104:119]\n" \
  "ds_read_b128 v[72:75], %[ws] offset:15360\n" \
  "v_mfma_f32_32x32x16_bf16 v[88:103], v[84:87], v[24:27], 0\n" \
  "ds_read_b128 v[84:87], %[ws] offset:16384\n" \
  "s_nop 8\n" \
  "v_cvt_pk_bf16_f32 v128, v104, v105\n" \
  "s_waitcnt lgkmcnt(2)\n" \
  "v_mfma_f32_32x32x16_bf16 v[88:103], v[76:79], v[28:31], v[88:103]\n" \
  "ds_read_b128 v[76:79], %[ws] offset:17408\n" \
  "v_cvt_pk_bf16_f32 v129, v106, v107\n" \
  "v_cvt_pk_bf16_f32 v130, v108, v109\n" \
  "v_cvt_pk_bf16_f32 v131, v110, v111\n" \
  "v_cvt_pk_bf16_f32 v132, v112, v113\n" \
  "v_cvt_pk_bf16_f32 v133, v114, v115\n" \
  "v_mfma_f32_32x32x16_bf16 v[88:103], v[80:83], v[32:35], v[88:103]\n" \
  "ds_read_b128 v[80:83], %[ws] offset:0\n" \
  "v_cvt_pk_bf16_f32 v134, v116, v117\n" \
  "v_cvt_pk_bf16_f32 v135, v118, v119\n" \
  "s_waitcnt lgkmcnt(2)\n" \
  "v_mfma_f32_32x32x16_bf16 v[88:103], v[72:75], v[36:39], v[88:103]\n" \
  "ds_read_b128 v[72:75], %[ws] offset:1024\n" \
  "v_mfma_f32_32x32x16_bf16 v[88:103], v[84:87], v[40:43], v[88:103]\n" \
  "ds_read_b128 v[84:87], %[ws] offset:2048\n" \
  "s_waitcnt lgkmcnt(2)\n" \
  "v_mfma_f32_32x32x16_bf16 v[88:103], v[76:79], v[44:47], v[88:103]\n" \
  "ds_read_b128 v[76:79], %[ws] offset:3072\n" \
  "v_mfma_f32_32x32x16_bf16 v[136:151], v[24:27], v[120:123], 0\n" \
  "s_nop 9\n" \
  "v_cvt_pk_bf16_f32 v152, v88, v89\n" \
  "v_mfma_f32_32x32x16_bf16 v[136:151], v[28:31], v[124:127], v[136:151]\n" \
  "v_cvt_pk_bf16_f32 v153, v90, v91\n" \
  "v_cvt_pk_bf16_f32 v154, v92, v93\n" \
  "v_cvt_pk_bf16_f32 v155, v94, v95\n" \
  "v_cvt_pk_bf16_f32 v156, v96, v97\n" \
  "v_cvt_pk_bf16_f32 v157, v98, v99\n" \
  "v_mfma_f32_32x32x16_bf16 v[136:151], v[32:35], v[128:131], v[136:151]\n" \
  "v_cvt_pk_bf16_f32 v158, v100, v101\n" \
  "v_cvt_pk_bf16_f32 v159, v102, v103\n" \
  "v_mfma_f32_32x32x16_bf16 v[136:151], v[36:39], v[132:135], v[136:151]\n" \
  "v_mfma_f32_32x32x16_bf16 v[136:151], v[40:43], v[152:155], v[136:151]\n" \
  "v_mfma_f32_32x32x16_bf16 v[136:151], v[44:47], v[156:159], v[136:151]\n" \
  "v_mfma_f32_32x32x16_bf16 v[160:175], v[48:51], v[120:123], 0\n" \
  "v_mfma_f32_32x32x16_bf16 v[160:175], v[52:55], v[124:127], v[160:175]\n" \
  "v_mfma_f32_32x32x16_bf16 v[160:175], v[56:59], v[128:131], v[160:175]\n" \
  "v_mfma_f32_32x32x16_bf16 v[160:175], v[60:63], v[132:135], v[160:175]\n" \
  "v_mfma_f32_32x32x16_bf16 v[160:175], v[64:67], v[152:155], v[160:175]\n" \
  "v_mfma_f32_32x32x16_bf16 v[160:175], v[68:71], v[156:159], v[160:175]\n" \
  "ds_read_b128 v[120:123], %[rka] offset:0\n" \
  "ds_read_b128 v[124:127], %[rka] offset:32\n" \
  "ds_read_b128 v[128:131], %[rka] offset:64\n" \
  "ds_read_b128 v[132:135], %[rka] offset:96\n" \
  "ds_read_b128 v[152:155], %[rka] offset:128\n" \
  "ds_read_b128 v[156:159], %[rka] offset:160\n" \
  "v_mfma_f32_32x32x16_bf16 v[104:119], v[80:83], v[48:51], 0\n" \
  "ds_read_b128 v[80:83], %[ws] offset:4096\n" \
  "ds_read_b32 v253, %[rka] offset:192\n" \
  "v_mov_b32_e32 v252, 0xff800000\n" \
  "v_cndmask_b32_e64 v251, 0, v252, s[66:67]\n" \
  "s_waitcnt lgkmcnt(8)\n" \
  "v_mfma_f32_32x32x16_bf16 v[104:119], v[72:75], v[52:55], v[104:119]\n" \
  "ds_read_b128 v[72:75], %[ws] offset:5120\n" \
  "s_waitcnt lgkmcnt(8)\n" \
  "v_mul_f32_e32 v136, v120, v136\n" \
  "v_mul_f32_e32 v137, v121, v137\n" \
  "v_mul_f32_e32 v138, v122, v138\n" \
  "v_mul_f32_e32 v139, v123, v139\n" \
  "s_waitcnt lgkmcnt(7)\n" \
  "v_mul_f32_e32 v140, v124, v140\n" \
  "v_max3_f32 v252, v136, v137, v138\n" \
  "v_mfma_f32_32x32x16_bf16 v[104:119], v[84:87], v[56:59], v[104:119]\n" \
  "ds_read_b128 v[84:87], %[ws] offset:6144\n" \
  "v_mul_f32_e32 v141, v125, v141\n" \
  "v_mul_f32_e32 v142, v126, v142\n" \
  "v_max3_f32 v252, v252, v139, v140\n" \
  "v_mul_f32_e32 v143, v127, v143\n" \
  "s_waitcnt lgkmcnt(4)\n" \
  "v_mul_f32_e32 v144, v128, v144\n" \
  "v_max3_f32 v252, v252, v141, v142\n" \
  "v_mfma_f32_32x32x16_bf16 v[104:119], v[76:79], v[60:63], v[104:119]\n" \
  "ds_read_b128 v[76:79], %[ws] offset:7168\n" \
  "v_mul_f32_e32 v145, v129, v145\n" \
  "v_mul_f32_e32 v146, v130, v146\n" \
  "v_max3_f32 v252, v252, v143, v144\n" \
  "v_mul_f32_e32 v147, v131, v147\n" \
  "v_mul_f32_e32 v148, v132, v148\n" \
  "v_max3_f32 v252, v252, v145, v146\n" \
  "s_waitcnt lgkmcnt(2)\n" \
  "v_mfma_f32_32x32x16_bf16 v[104:119], v[80:83], v[64:67], v[104:119]\n" \
  "ds_read_b128 v[80:83], %[ws] offset:8192\n" \
  "v_mul_f32_e32 v149, v133, v149\n" \
  "v_mul_f32_e32 v150, v134, v150\n" \
  "v_max3_f32 v252, v252, v147, v148\n" \
  "v_mul_f32_e32 v151, v135, v151\n" \
  "v_mul_f32_e32 v160, v152, v160\n" \
  "v_max3_f32 v252, v252, v149, v150\n" \
  "v_mfma_f32_32x32x16_bf16 v[104:119], v[72:75], v[68:71], v[104:119]\n" \
  "ds_read_b128 v[72:75], %[ws] offset:9216\n" \
  "v_mul_f32_e32 v161, v153, v161\n" \
  "v_mul_f32_e32 v162, v154, v162\n" \
  "v_max3_f32 v252, v252, v151, v160\n" \
  "v_mul_f32_e32 v163, v155, v163\n" \
  "v_mul_f32_e32 v164, v156, v164\n" \
  "v_max3_f32 v252, v252, v161, v162\n" \
  "s_waitcnt lgkmcnt(2)\n" \
  "v_mfma_f32_32x32x16_bf16 v[88:103], v[84:87], v[48:51], 0\n" \
  "ds_read_b128 v[84:87], %[ws] offset:10240\n" \
  "v_mul_f32_e32 v165, v157, v165\n" \
  "v_mul_f32_e32 v166, v158, v166\n" \
  "v_max3_f32 v252, v252, v163, v164\n" \
  "v_mul_f32_e32 v167, v159, v167\n" \
  "v_cvt_pk_bf16_f32 v176, v104, v105\n" \
  "v_cvt_pk_bf16_f32 v177, v106, v107\n" \
  "v_mfma_f32_32x32x16_bf16 v[88:103], v[76:79], v[52:55], v[88:103]\n" \
  "ds_read_b128 v[76:79], %[ws] offset:11264\n" \
  "v_cvt_pk_bf16_f32 v178, v108, v109\n" \
  "v_cvt_pk_bf16_f32 v179, v110, v111\n" \
  "v_cvt_pk_bf16_f32 v180, v112, v113\n" \
  "v_cvt_pk_bf16_f32 v181, v114, v115\n" \
  "v_cvt_pk_bf16_f32 v182, v116, v117\n" \
  "s_waitcnt lgkmcnt(2)\n" \
  "v_mfma_f32_32x32x16_bf16 v[88:103], v[80:83], v[56:59], v[88:103]\n" \
  "ds_read_b128 v[80:83], %[ws] offset:12288\n" \
  "v_cvt_pk_bf16_f32 v183, v118, v119\n" \
  "v_fma_f32 v168, v168, v253, v251\n" \
  "v_max3_f32 v252, v252, v165, v166\n" \
  "v_max3_f32 v252, v252, v167, v168\n" \
  "v_mov_b32_e32 v250, v252\n" \
  "v_mfma_f32_32x32x16_bf16 v[88:103], v[72:75], v[60:63], v[88:103]\n" \
  "ds_read_b128 v[72:75], %[ws] offset:13312\n" \
  "v_permlane32_swap_b32_e32 v252, v250\n" \
  "v_max_f32_e32 v252, v252, v250\n" \
  "v_mul_f32_e64 v250, -v252, v255\n" \
  "v_fma_f32 v136, v136, v255, v250\n" \
  "s_waitcnt lgkmcnt(2)\n" \
  "v_mfma_f32_32x32x16_bf16 v[88:103], v[84:87], v[64:67], v[88:103]\n" \
  "ds_read_b128 v[84:87], %[ws] offset:14336\n" \
  "v_fma_f32 v137, v137, v255, v250\n" \
  "v_exp_f32_e32 v136, v136\n" \
  "v_exp_f32_e32 v137, v137\n" \
  "v_fma_f32 v138, v138, v255, v250\n" \
  "v_mfma_f32_32x32x16_bf16 v[88:103], v[76:79], v[68:71], v[88:103]\n" \
  "ds_read_b128 v[76:79], %[ws] offset:15360\n" \
  "v_fma_f32 v139, v139, v255, v250\n" \
  "v_fma_f32 v140, v140, v255, v250\n" \
  "v_exp_f32_e32 v138, v138\n" \
  "v_exp_f32_e32 v139, v139\n" \
  "v_exp_f32_e32 v140, v140\n" \
  "v_fma_f32 v141, v141, v255, v250\n" \
  "s_waitcnt lgkmcnt(2)\n" \
  "v_mfma_f32_32x32x16_bf16 v[104:119], v[80:83], v[48:51], 0\n" \
  "ds_read_b128 v[80:83], %[ws] offset:16384\n" \
  "v_fma_f32 v142, v142, v255, v250\n" \
  "v_exp_f32_e32 v141, v141\n" \
  "v_exp_f32_e32 v142, v142\n" \
  "v_fma_f32 v143, v143, v255, v250\n" \
  "v_mfma_f32_32x32x16_bf16 v[104:119], v[72:75], v[52:55], v[104:119]\n" \
  "ds_read_b128 v[72:75], %[ws] offset:17408\n" \
  "v_fma_f32 v144, v144, v255, v250\n" \
  "v_add_f32_e32 v252, v136, v137\n" \
  "v_exp_f32_e32 v143, v143\n" \
  "v_exp_f32_e32 v144, v144\n" \
  "v_fma_f32 v145, v145, v255, v250\n" \
  "s_waitcnt lgkmcnt(2)\n" \
  "v_mfma_f32_32x32x16_bf16 v[104:119], v[84:87], v[56:59], v[104:119]\n" \
  "ds_read_b128 v[84:87], %[ws] offset:18432\n" \
  "v_fma_f32 v146, v146, v255, v250\n" \
  "v_add_f32_e32 v249, v138, v139\n" \
  "v_add_f32_e32 v252, v252, v140\n" \
  "v_exp_f32_e32 v145, v145\n" \
  "v_exp_f32_e32 v146, v146\n" \
  "v_fma_f32 v147, v147, v255, v250\n" \
  "v_mfma_f32_32x32x16_bf16 v[104:119], v[76:79], v[60:63], v[104:119]\n" \
  "ds_read_b128 v[76:79], %[ws] offset:19456\n" \
  "v_fma_f32 v148, v148, v255, v250\n" \
  "v_cvt_pk_bf16_f32 v184, v88, v89\n" \
  "v_cvt_pk_bf16_f32 v185, v90, v91\n" \
  "v_cvt_pk_bf16_f32 v186, v92, v93\n" \
  "v_cvt_pk_bf16_f32 v187, v94, v95\n" \
  "s_waitcnt lgkmcnt(2)\n" \
  "v_mfma_f32_32x32x16_bf16 v[104:119], v[80:83], v[64:67], v[104:119]\n" \
  "ds_read_b128 v[80:83], %[ws] offset:20480\n" \
  "v_add_f32_e32 v249, v249, v141\n" \
  "v_add_f32_e32 v252, v252, v142\n" \
  "v_exp_f32_e32 v147, v147\n" \
  "v_exp_f32_e32 v148, v148\n" \
  "v_fma_f32 v149, v149, v255, v250\n" \
  "v_mfma_f32_32x32x16_bf16 v[104:119], v[72:75], v[68:71], v[104:119]\n" \
  "ds_read_b128 v[72:75], %[ws] offset:21504\n" \
  "v_fma_f32 v150, v150, v255, v250\n" \
  "v_add_f32_e32 v249, v249, v143\n" \
  "v_add_f32_e32 v252, v252, v144\n" \
  "v_exp_f32_e32 v149, v149\n" \
  "v_exp_f32_e32 v150, v150\n" \
  "v_fma_f32 v151, v151, v255, v250\n" \
  "v_mfma_f32_32x32x16_bf16 v[188:203], v[24:27], v[176:179], 0\n" \
  "v_exp_f32_e32 v151, v151\n" \
  "v_fma_f32 v160, v160, v255, v250\n" \
  "v_add_f32_e32 v249, v249, v145\n" \
  "v_add_f32_e32 v252, v252, v146\n" \
  "v_exp_f32_e32 v160, v160\n" \
  "v_fma_f32 v161, v161, v255, v250\n" \
  "v_mfma_f32_32x32x16_bf16 v[188:203], v[28:31], v[180:183], v[188:203]\n" \
  "v_exp_f32_e32 v161, v161\n" \
  "v_fma_f32 v162, v162, v255, v250\n" \
  "v_add_f32_e32 v249, v249, v147\n" \
  "v_add_f32_e32 v252, v252, v148\n" \
  "v_exp_f32_e32 v162, v162\n" \
  "v_fma_f32 v163, v163, v255, v250\n" \
  "v_mfma_f32_32x32x16_bf16 v[188:203], v[32:35], v[184:187], v[188:203]\n" \
  "v_exp_f32_e32 v163, v163\n" \
  "v_fma_f32 v164, v164, v255, v250\n" \
  "v_add_f32_e32 v249, v249, v149\n" \
  "v_add_f32_e32 v252, v252, v150\n" \
  "v_exp_f32_e32 v164, v164\n" \
  "v_fma_f32 v165, v165, v255, v250\n" \
  "v_mfma_f32_32x32x16_bf16 v[204:219], v[48:51], v[176:179], 0\n" \
  "v_exp_f32_e32 v165, v165\n" \
  "v_fma_f32 v166, v166, v255, v250\n" \
  "v_add_f32_e32 v249, v249, v151\n" \
  "v_add_f32_e32 v252, v252, v160\n" \
  "v_exp_f32_e32 v166, v166\n" \
  "v_fma_f32 v167, v167, v255, v250\n" \
  "v_mfma_f32_32x32x16_bf16 v[204:219], v[52:55], v[180:183], v[204:219]\n" \
  "v_exp_f32_e32 v167, v167\n" \
  "v_fma_f32 v168, v168, v255, v250\n" \
  "v_add_f32_e32 v249, v249, v161\n" \
  "v_add_f32_e32 v252, v252, v162\n" \
  "v_cvt_pk_bf16_f32 v176, v96, v97\n" \
  "v_mfma_f32_32x32x16_bf16 v[204:219], v[56:59], v[184:187], v[204:219]\n" \
  "v_exp_f32_e32 v168, v168\n" \
  "v_add_f32_e32 v249, v249, v163\n" \
  "v_cvt_pk_bf16_f32 v177, v98, v99\n" \
  "v_cvt_pk_bf16_f32 v178, v100, v101\n" \
  "v_cvt_pk_bf16_f32 v179, v102, v103\n" \
  "v_add_f32_e32 v252, v252, v164\n" \
  "v_add_f32_e32 v249, v249, v165\n" \
  "v_mfma_f32_32x32x16_bf16 v[188:203], v[36:39], v[176:179], v[188:203]\n" \
  "v_add_f32_e32 v252, v252, v166\n" \
  "v_add_f32_e32 v249, v249, v167\n" \
  "v_add_f32_e32 v252, v252, v168\n" \
  "v_add_f32_e32 v252, v252, v249\n" \
  "v_mov_b32_e32 v250, v252\n" \
  "v_cvt_pk_bf16_f32 v180, v104, v105\n" \
  "v_mfma_f32_32x32x16_bf16 v[204:219], v[60:63], v[176:179], v[204:219]\n" \
  "v_mul_f32_e32 v136, v120, v136\n" \
  "v_cvt_pk_bf16_f32 v181, v106, v107\n" \
  "v_cvt_pk_bf16_f32 v182, v108, v109\n" \
  "v_cvt_pk_bf16_f32 v183, v110, v111\n" \
  "v_permlane32_swap_b32_e32 v252, v250\n" \
  "v_mul_f32_e32 v137, v121, v137\n" \
  "v_mfma_f32_32x32x16_bf16 v[188:203], v[40:43], v[180:183], v[188:203]\n" \
  "v_mul_f32_e32 v138, v122, v138\n" \
  "v_add_f32_e32 v252, v252, v250\n" \
  "v_rcp_f32_e32 v250, v252\n" \
  "v_mul_f32_e32 v139, v123, v139\n" \
  "v_mul_f32_e32 v168, v253, v168\n" \
  "v_mfma_f32_32x32x16_bf16 v[204:219], v[64:67], v[180:183], v[204:219]\n" \
  "v_mul_f32_e32 v140, v124, v140\n" \
  "v_cvt_pk_bf16_f32 v176, v112, v113\n" \
  "v_cvt_pk_bf16_f32 v177, v114, v115\n" \
  "v_cvt_pk_bf16_f32 v178, v116, v117\n" \
  "v_cvt_pk_bf16_f32 v179, v118, v119\n" \
  "s_waitcnt lgkmcnt(0)\n" \
  "v_mfma_f32_32x32x16_bf16 v[104:119], v[48:51], v[84:87], 0\n" \
  "ds_read_b128 v[180:183], %[ws] offset:22528\n" \
  "v_mul_f32_e32 v168, v250, v168\n" \
  "v_cvt_pk_bf16_f32 v184, v168, 0\n" \
  "v_mov_b32_e32 v185, 0\n" \
  "v_mul_f32_e32 v141, v125, v141\n" \
  "v_mul_f32_e32 v142, v126, v142\n" \
  "v_mul_f32_e32 v143, v127, v143\n" \
  "v_mfma_f32_32x32x16_bf16 v[188:203], v[44:47], v[176:179], v[188:203]\n" \
  "v_mul_f32_e32 v144, v128, v144\n" \
  "v_mul_f32_e32 v136, v250, v136\n" \
  "v_mul_f32_e32 v137, v250, v137\n" \
  "v_mul_f32_e32 v138, v250, v138\n" \
  "v_mul_f32_e32 v139, v250, v139\n" \
  "v_mul_f32_e32 v140, v250, v140\n" \
  "v_mfma_f32_32x32x16_bf16 v[204:219], v[68:71], v[176:179], v[204:219]\n" \
  "v_mul_f32_e32 v145, v129, v145\n" \
  "v_mul_f32_e32 v141, v250, v141\n" \
  "v_mul_f32_e32 v142, v250, v142\n" \
  "v_mul_f32_e32 v143, v250, v143\n" \
  "v_cvt_pk_bf16_f32 v176, v136, v137\n" \
  "v_mul_f32_e32 v188, v120, v188\n" \
  "v_mfma_f32_32x32x16_bf16 v[88:103], v[24:27], v[84:87], 0\n" \
  "v_mul_f32_e32 v146, v130, v146\n" \
  "v_mul_f32_e32 v189, v121, v189\n" \
  "v_mul_f32_e32 v190, v122, v190\n" \
  "v_mul_f32_e32 v191, v123, v191\n" \
  "v_mul_f32_e32 v192, v124, v192\n" \
  "v_max3_f32 v252, v188, v189, v190\n" \
  "v_mfma_f32_32x32x16_bf16 v[88:103], v[28:31], v[76:79], v[88:103]\n" \
  "v_mul_f32_e32 v147, v131, v147\n" \
  "v_mul_f32_e32 v193, v125, v193\n" \
  "v_mul_f32_e32 v194, v126, v194\n" \
  "v_max3_f32 v252, v252, v191, v192\n" \
  "v_mul_f32_e32 v195, v127, v195\n" \
  "v_mul_f32_e32 v196, v128, v196\n" \
  "v_mfma_f32_32x32x16_bf16 v[104:119], v[52:55], v[76:79], v[104:119]\n" \
  "ds_read_b128 v[76:79], %[ws] offset:23552\n" \
  "v_max3_f32 v252, v252, v193, v194\n" \
  "v_mul_f32_e32 v197, v129, v197\n" \
  "v_mul_f32_e32 v198, v130, v198\n" \
  "v_max3_f32 v252, v252, v195, v196\n" \
  "v_mul_f32_e32 v199, v131, v199\n" \
  "v_mul_f32_e32 v200, v132, v200\n" \
  "v_mfma_f32_32x32x16_bf16 v[88:103], v[32:35], v[80:83], v[88:103]\n" \
  "v_mul_f32_e32 v148, v132, v148\n" \
  "v_max3_f32 v252, v252, v197, v198\n" \
  "v_mul_f32_e32 v201, v133, v201\n" \
  "v_mul_f32_e32 v202, v134, v202\n" \
  "v_max3_f32 v252, v252, v199, v200\n" \
  "v_mul_f32_e32 v203, v135, v203\n" \
  "v_mfma_f32_32x32x16_bf16 v[104:119], v[56:59], v[80:83], v[104:119]\n" \
  "ds_read_b128 v[80:83], %[ws] offset:24576\n" \
  "v_mul_f32_e32 v204, v152, v204\n" \
  "v_max3_f32 v252, v252, v201, v202\n" \
  "v_mul_f32_e32 v205, v153, v205\n" \
  "v_mul_f32_e32 v206, v154, v206\n" \
  "v_max3_f32 v252, v252, v203, v204\n" \
  "v_cvt_pk_bf16_f32 v177, v138, v139\n" \
  "v_mfma_f32_32x32x16_bf16 v[88:103], v[36:39], v[72:75], v[88:103]\n" \
  "v_mul_f32_e32 v149, v133, v149\n" \
  "v_cvt_pk_bf16_f32 v178, v140, v141\n" \
  "v_cvt_pk_bf16_f32 v179, v142, v143\n" \
  "v_mul_f32_e32 v207, v155, v207\n" \
  "v_mul_f32_e32 v208, v156, v208\n" \
  "v_max3_f32 v252, v252, v205, v206\n" \
  "v_mfma_f32_32x32x16_bf16 v[104:119], v[60:63], v[72:75], v[104:119]\n" \
  "ds_read_b128 v[72:75], %[ws] offset:25600\n" \
  "v_mul_f32_e32 v209, v157, v209\n" \
  "v_mul_f32_e32 v210, v158, v210\n" \
  "v_max3_f32 v252, v252, v207, v208\n" \
  "v_mul_f32_e32 v211, v159, v211\n" \
  "v_fma_f32 v212, v212, v253, v251\n" \
  "v_max3_f32 v252, v252, v209, v210\n" \
  "s_waitcnt lgkmcnt(1)\n" \
  "v_mfma_f32_32x32x16_bf16 v[88:103], v[40:43], v[180:183], v[88:103]\n" \
  "v_mul_f32_e32 v150, v134, v150\n" \
  "v_max3_f32 v252, v252, v211, v212\n" \
  "v_mov_b32_e32 v251, v252\n" \
  "v_mul_f32_e32 v151, v135, v151\n" \
  "v_mul_f32_e32 v144, v250, v144\n" \
  "v_mul_f32_e32 v145, v250, v145\n" \
  "v_mfma_f32_32x32x16_bf16 v[104:119], v[64:67], v[180:183], v[104:119]\n" \
  "ds_read_b128 v[84:87], %[ws] offset:26624\n" \
  "v_mul_f32_e32 v146, v250, v146\n" \
  "v_mul_f32_e32 v147, v250, v147\n" \
  "v_mul_f32_e32 v148, v250, v148\n" \
  "v_mul_f32_e32 v149, v250, v149\n" \
  "v_mul_f32_e32 v150, v250, v150\n" \
  "v_mul_f32_e32 v151, v250, v151\n" \
  "v_mfma_f32_32x32x16_bf16 v[88:103], v[44:47], v[76:79], v[88:103]\n" \
  "v_mul_f32_e32 v160, v152, v160\n" \
  "v_cvt_pk_bf16_f32 v180, v144, v145\n" \
  "v_cvt_pk_bf16_f32 v181, v146, v147\n" \
  "v_cvt_pk_bf16_f32 v182, v148, v149\n" \
  "v_cvt_pk_bf16_f32 v183, v150, v151\n" \
  "v_mfma_f32_32x32x16_bf16 v[104:119], v[68:71], v[76:79], v[104:119]\n" \
  "ds_read_b128 v[76:79], %[ws] offset:27648\n" \
  "v_permlane32_swap_b32_e32 v252, v251\n" \
  "v_mul_f32_e32 v161, v153, v161\n" \
  "v_max_f32_e32 v252, v252, v251\n" \
  "v_mul_f32_e64 v251, -v252, v254\n" \
  "s_nop 0\n" \
  "v_cvt_pk_bf16_f32 v136, v88, v89\n" \
  "v_cvt_pk_bf16_f32 v137, v90, v91\n" \
  "v_cvt_pk_bf16_f32 v138, v92, v93\n" \
  "v_cvt_pk_bf16_f32 v139, v94, v95\n" \
  "v_cvt_pk_bf16_f32 v140, v96, v97\n" \
  "v_cvt_pk_bf16_f32 v141, v98, v99\n" \
  "v_cvt_pk_bf16_f32 v142, v100, v101\n" \
  "v_cvt_pk_bf16_f32 v143, v102, v103\n" \
  "v_mfma_f32_32x32x16_bf16 v[88:103], v[24:27], v[80:83], 0\n" \
  "v_mul_f32_e32 v162, v154, v162\n" \
  "v_cvt_pk_bf16_f32 v144, v104, v105\n" \
  "v_cvt_pk_bf16_f32 v145, v106, v107\n" \
  "v_cvt_pk_bf16_f32 v146, v108, v109\n" \
  "v_cvt_pk_bf16_f32 v147, v110, v111\n" \
  "s_waitcnt lgkmcnt(0)\n" \
  "v_mfma_f32_32x32x16_bf16 v[88:103], v[28:31], v[72:75], v[88:103]\n" \
  "v_mul_f32_e32 v163, v155, v163\n" \
  "v_cvt_pk_bf16_f32 v148, v112, v113\n" \
  "v_cvt_pk_bf16_f32 v149, v114, v115\n" \
  "v_mul_f32_e32 v164, v156, v164\n" \
  "v_mul_f32_e32 v165, v157, v165\n" \
  "v_mul_f32_e32 v166, v158, v166\n" \
  "v_mfma_f32_32x32x16_bf16 v[104:119], v[48:51], v[80:83], 0\n" \
  "ds_read_b128 v[80:83], %[ws] offset:28672\n" \
  "v_mul_f32_e32 v167, v159, v167\n" \
  "v_fma_f32 v188, v188, v254, v251\n" \
  "v_fma_f32 v189, v189, v254, v251\n" \
  "v_mul_f32_e32 v160, v250, v160\n" \
  "v_mul_f32_e32 v161, v250, v161\n" \
  "v_mul_f32_e32 v162, v250, v162\n" \
  "v_mfma_f32_32x32x16_bf16 v[104:119], v[52:55], v[72:75], v[104:119]\n" \
  "ds_read_b128 v[72:75], %[ws] offset:29696\n" \
  "v_mul_f32_e32 v163, v250, v163\n" \
  "v_mul_f32_e32 v164, v250, v164\n" \
  "v_mul_f32_e32 v165, v250, v165\n" \
  "v_mul_f32_e32 v166, v250, v166\n" \
  "v_mul_f32_e32 v167, v250, v167\n" \
  "v_exp_f32_e32 v188, v188\n" \
  "v_cvt_pk_bf16_f32 v220, v160, v161\n" \
  "v_mfma_f32_32x32x16_bf16 v[88:103], v[32:35], v[84:87], v[88:103]\n" \
  "v_cvt_pk_bf16_f32 v221, v162, v163\n" \
  "v_exp_f32_e32 v189, v189\n" \
  "v_cvt_pk_bf16_f32 v222, v164, v165\n" \
  "v_fma_f32 v190, v190, v254, v251\n" \
  "v_fma_f32 v191, v191, v254, v251\n" \
  "v_mfma_f32_32x32x16_bf16 v[104:119], v[56:59], v[84:87], v[104:119]\n" \
  "ds_read_b128 v[84:87], %[ws] offset:30720\n" \
  "v_fma_f32 v192, v192, v254, v251\n" \
  "v_cvt_pk_bf16_f32 v223, v166, v167\n" \
  "v_exp_f32_e32 v190, v190\n" \
  "v_exp_f32_e32 v191, v191\n" \
  "v_mfma_f32_32x32x16_bf16 v[88:103], v[36:39], v[76:79], v[88:103]\n" \
  "v_exp_f32_e32 v192, v192\n" \
  "v_fma_f32 v193, v193, v254, v251\n" \
  "v_fma_f32 v194, v194, v254, v251\n" \
  "v_exp_f32_e32 v193, v193\n" \
  "v_mfma_f32_32x32x16_bf16 v[104:119], v[60:63], v[76:79], v[104:119]\n" \
  "ds_read_b128 v[76:79], %[ws] offset:31744\n" \
  "v_exp_f32_e32 v194, v194\n" \
  "v_fma_f32 v195, v195, v254, v251\n" \
  "v_fma_f32 v196, v196, v254, v251\n" \
  "v_add_f32_e32 v252, v188, v189\n" \
  "v_exp_f32_e32 v195, v195\n" \
  "s_waitcnt lgkmcnt(1)\n" \
  "v_mfma_f32_32x32x16_bf16 v[88:103], v[40:43], v[80:83], v[88:103]\n" \
  "v_exp_f32_e32 v196, v196\n" \
  "v_fma_f32 v197, v197, v254, v251\n" \
  "v_fma_f32 v198, v198, v254, v251\n" \
  "v_add_f32_e32 v250, v190, v191\n" \
  "v_add_f32_e32 v252, v252, v192\n" \
  "v_mfma_f32_32x32x16_bf16 v[104:119], v[64:67], v[80:83], v[104:119]\n" \
  "ds_read_b128 v[80:83], %[ws] offset:32768\n" \
  "v_exp_f32_e32 v197, v197\n" \
  "v_exp_f32_e32 v198, v198\n" \
  "v_fma_f32 v199, v199, v254, v251\n" \
  "v_fma_f32 v200, v200, v254, v251\n" \
  "v_mfma_f32_32x32x16_bf16 v[88:103], v[44:47], v[72:75], v[88:103]\n" \
  "v_exp_f32_e32 v199, v199\n" \
  "v_exp_f32_e32 v200, v200\n" \
  "v_fma_f32 v201, v201, v254, v251\n" \
  "v_add_f32_e32 v250, v250, v193\n" \
  "v_mfma_f32_32x32x16_bf16 v[104:119], v[68:71], v[72:75], v[104:119]\n" \
  "ds_read_b128 v[72:75], %[ws] offset:33792\n" \
  "v_add_f32_e32 v252, v252, v194\n" \
  "v_fma_f32 v202, v202, v254, v251\n" \
  "v_add_f32_e32 v250, v250, v195\n" \
  "v_add_f32_e32 v252, v252, v196\n" \
  "v_exp_f32_e32 v201, v201\n" \
  "s_nop 0\n" \
  "v_cvt_pk_bf16_f32 v160, v88, v89\n" \
  "v_cvt_pk_bf16_f32 v161, v90, v91\n" \
  "v_cvt_pk_bf16_f32 v162, v92, v93\n" \
  "v_cvt_pk_bf16_f32 v163, v94, v95\n" \
  "v_cvt_pk_bf16_f32 v164, v96, v97\n" \
  "v_cvt_pk_bf16_f32 v165, v98, v99\n" \
  "v_cvt_pk_bf16_f32 v166, v100, v101\n" \
  "v_cvt_pk_bf16_f32 v167, v102, v103\n" \
  "v_mfma_f32_32x32x16_bf16 v[88:103], v[24:27], v[84:87], 0\n" \
  "v_cvt_pk_bf16_f32 v168, v104, v105\n" \
  "v_cvt_pk_bf16_f32 v169, v106, v107\n" \
  "v_cvt_pk_bf16_f32 v170, v108, v109\n" \
  "v_cvt_pk_bf16_f32 v171, v110, v111\n" \
  "v_cvt_pk_bf16_f32 v150, v112, v113\n" \
  "s_waitcnt lgkmcnt(0)\n" \
  "v_mfma_f32_32x32x16_bf16 v[88:103], v[28:31], v[76:79], v[88:103]\n" \
  "v_cvt_pk_bf16_f32 v151, v114, v115\n" \
  "v_exp_f32_e32 v202, v202\n" \
  "v_fma_f32 v203, v203, v254, v251\n" \
  "v_fma_f32 v204, v204, v254, v251\n" \
  "v_add_f32_e32 v250, v250, v197\n" \
  "v_mfma_f32_32x32x16_bf16 v[104:119], v[48:51], v[84:87], 0\n" \
  "ds_read_b128 v[84:87], %[ws] offset:34816\n" \
  "v_add_f32_e32 v252, v252, v198\n" \
  "v_exp_f32_e32 v203, v203\n" \
  "v_exp_f32_e32 v204, v204\n" \
  "v_fma_f32 v205, v205, v254, v251\n" \
  "v_mfma_f32_32x32x16_bf16 v[104:119], v[52:55], v[76:79], v[104:119]\n" \
  "ds_read_b128 v[76:79], %[ws] offset:35840\n" \
  "v_fma_f32 v206, v206, v254, v251\n" \
  "v_add_f32_e32 v250, v250, v199\n" \
  "v_add_f32_e32 v252, v252, v200\n" \
  "v_exp_f32_e32 v205, v205\n" \
  "v_exp_f32_e32 v206, v206\n" \
  "v_mfma_f32_32x32x16_bf16 v[88:103], v[32:35], v[80:83], v[88:103]\n" \
  "v_fma_f32 v207, v207, v254, v251\n" \
  "v_fma_f32 v208, v208, v254, v251\n" \
  "v_add_f32_e32 v250, v250, v201\n" \
  "v_add_f32_e32 v252, v252, v202\n" \
  "v_exp_f32_e32 v207, v207\n" \
  "v_mfma_f32_32x32x16_bf16 v[104:119], v[56:59], v[80:83], v[104:119]\n" \
  "v_exp_f32_e32 v208, v208\n" \
  "v_fma_f32 v209, v209, v254, v251\n" \
  "v_fma_f32 v210, v210, v254, v251\n" \
  "v_add_f32_e32 v250, v250, v203\n" \
  "v_add_f32_e32 v252, v252, v204\n" \
  "v_mfma_f32_32x32x16_bf16 v[88:103], v[36:39], v[72:75], v[88:103]\n" \
  "v_exp_f32_e32 v209, v209\n" \
  "v_exp_f32_e32 v210, v210\n" \
  "v_fma_f32 v211, v211, v254, v251\n" \
  "v_fma_f32 v212, v212, v254, v251\n" \
  "v_mfma_f32_32x32x16_bf16 v[104:119], v[60:63], v[72:75], v[104:119]\n" \
  "v_exp_f32_e32 v211, v211\n" \
  "v_exp_f32_e32 v212, v212\n" \
  "v_add_f32_e32 v250, v250, v205\n" \
  "v_add_f32_e32 v252, v252, v206\n" \
  "s_waitcnt lgkmcnt(0)\n" \
  "v_mfma_f32_32x32x16_bf16 v[88:103], v[40:43], v[84:87], v[88:103]\n" \
  "v_add_f32_e32 v250, v250, v207\n" \
  "v_add_f32_e32 v252, v252, v208\n" \
  "v_add_f32_e32 v250, v250, v209\n" \
  "v_add_f32_e32 v252, v252, v210\n" \
  "v_add_f32_e32 v250, v250, v211\n" \
  "v_add_f32_e32 v252, v252, v212\n" \
  "v_mfma_f32_32x32x16_bf16 v[104:119], v[64:67], v[84:87], v[104:119]\n" \
  "v_add_f32_e32 v252, v252, v250\n" \
  "v_mov_b32_e32 v251, v252\n" \
  "v_mul_f32_e32 v188, v120, v188\n" \
  "v_mul_f32_e32 v189, v121, v189\n" \
  "v_permlane32_swap_b32_e32 v252, v251\n" \
  "v_mfma_f32_32x32x16_bf16 v[88:103], v[44:47], v[76:79], v[88:103]\n" \
  "v_mul_f32_e32 v190, v122, v190\n" \
  "v_add_f32_e32 v252, v252, v251\n" \
  "v_rcp_f32_e32 v251, v252\n" \
  "v_mul_f32_e32 v191, v123, v191\n" \
  "v_mul_f32_e32 v192, v124, v192\n" \
  "v_mfma_f32_32x32x16_bf16 v[104:119], v[68:71], v[76:79], v[104:119]\n" \
  "v_mul_f32_e32 v193, v125, v193\n" \
  "v_mul_f32_e32 v194, v126, v194\n" \
  "v_mul_f32_e32 v195, v127, v195\n" \
  "v_mul_f32_e32 v188, v251, v188\n" \
  "v_mul_f32_e32 v189, v251, v189\n" \
  "s_nop 0\n" \
  "v_cvt_pk_bf16_f32 v72, v88, v89\n" \
  "v_cvt_pk_bf16_f32 v73, v90, v91\n" \
  "v_cvt_pk_bf16_f32 v74, v92, v93\n" \
  "v_cvt_pk_bf16_f32 v75, v94, v95\n" \
  "v_cvt_pk_bf16_f32 v76, v96, v97\n" \
  "v_cvt_pk_bf16_f32 v77, v98, v99\n" \
  "v_cvt_pk_bf16_f32 v78, v100, v101\n" \
  "v_cvt_pk_bf16_f32 v79, v102, v103\n" \
  "v_lshlrev_b32_e32 v88, 16, v24\n" \
  "v_and_b32_e32 v89, s64, v24\n" \
  "v_lshlrev_b32_e32 v90, 16, v25\n" \
  "v_and_b32_e32 v91, s64, v25\n" \
  "v_lshlrev_b32_e32 v92, 16, v26\n" \
  "v_and_b32_e32 v93, s64, v26\n" \
  "v_lshlrev_b32_e32 v94, 16, v27\n" \
  "v_and_b32_e32 v95, s64, v27\n" \
  "v_lshlrev_b32_e32 v96, 16, v28\n" \
  "v_and_b32_e32 v97, s64, v28\n" \
  "v_lshlrev_b32_e32 v98, 16, v29\n" \
  "v_and_b32_e32 v99, s64, v29\n" \
  "v_lshlrev_b32_e32 v100, 16, v30\n" \
  "v_and_b32_e32 v101, s64, v30\n" \
  "v_lshlrev_b32_e32 v102, 16, v31\n" \
  "v_and_b32_e32 v103, s64, v31\n" \
  "v_cvt_pk_bf16_f32 v24, v104, v105\n" \
  "v_cvt_pk_bf16_f32 v25, v106, v107\n" \
  "v_mfma_f32_32x32x16_bf16 v[88:103], v[136:139], v[176:179], v[88:103]\n" \
  "v_cvt_pk_bf16_f32 v26, v108, v109\n" \
  "v_cvt_pk_bf16_f32 v27, v110, v111\n" \
  "v_cvt_pk_bf16_f32 v28, v112, v113\n" \
  "v_cvt_pk_bf16_f32 v29, v114, v115\n" \
  "v_lshlrev_b32_e32 v104, 16, v32\n" \
  "v_mfma_f32_32x32x16_bf16 v[88:103], v[140:143], v[180:183], v[88:103]\n" \
  "v_mul_f32_e32 v196, v128, v196\n" \
  "v_and_b32_e32 v105, s64, v32\n" \
  "v_lshlrev_b32_e32 v106, 16, v33\n" \
  "v_and_b32_e32 v107, s64, v33\n" \
  "v_lshlrev_b32_e32 v108, 16, v34\n" \
  "v_and_b32_e32 v109, s64, v34\n" \
  "v_mfma_f32_32x32x16_bf16 v[88:103], v[144:147], v[220:223], v[88:103]\n" \
  "v_mul_f32_e32 v197, v129, v197\n" \
  "v_lshlrev_b32_e32 v110, 16, v35\n" \
  "v_and_b32_e32 v111, s64, v35\n" \
  "v_lshlrev_b32_e32 v112, 16, v36\n" \
  "v_and_b32_e32 v113, s64, v36\n" \
  "v_lshlrev_b32_e32 v114, 16, v37\n" \
  "v_mfma_f32_32x32x8_bf16 v[88:103], v[148:149], v[184:185], v[88:103]\n" \
  "v_mul_f32_e32 v198, v130, v198\n" \
  "v_and_b32_e32 v115, s64, v37\n" \
  "v_lshlrev_b32_e32 v116, 16, v38\n" \
  "v_and_b32_e32 v117, s64, v38\n" \
  "v_lshlrev_b32_e32 v118, 16, v39\n" \
  "v_and_b32_e32 v119, s64, v39\n" \
  "v_mul_f32_e32 v190, v251, v190\n" \
  "v_mul_f32_e32 v191, v251, v191\n" \
  "v_mfma_f32_32x32x16_bf16 v[104:119], v[160:163], v[176:179], v[104:119]\n" \
  "v_mul_f32_e32 v199, v131, v199\n" \
  "v_mul_f32_e32 v192, v251, v192\n" \
  "v_mul_f32_e32 v193, v251, v193\n" \
  "v_cvt_pk_bf16_f32 v30, v88, v89\n" \
  "v_cvt_pk_bf16_f32 v31, v90, v91\n" \
  "v_cvt_pk_bf16_f32 v32, v92, v93\n" \
  "v_mfma_f32_32x32x16_bf16 v[104:119], v[164:167], v[180:183], v[104:119]\n" \
  "v_mul_f32_e32 v200, v132, v200\n" \
  "v_cvt_pk_bf16_f32 v33, v94, v95\n" \
  "ds_write_b128 %[h0e], v[30:33] offset:0\n" \
  "s_waitcnt vmcnt(0)\n" \
  "s_mov_b64 s[72:73], exec\n" \
  "s_mov_b64 exec, 1\n" \
  "ds_add_u32 %[cnt], %[flag] offset:0\n" \
  "s_mov_b64 exec, s[72:73]\n" \
  "ds_read_b32 v255, %[tab] offset:1024\n" \
  "v_mfma_f32_32x32x16_bf16 v[104:119], v[168:171], v[220:223], v[104:119]\n" \
  "ds_read_b32 v254, %[tab] offset:1280\n" \
  "v_cvt_pk_bf16_f32 v30, v96, v97\n" \
  "v_cvt_pk_bf16_f32 v31, v98, v99\n" \
  "v_cvt_pk_bf16_f32 v32, v100, v101\n" \
  "v_cvt_pk_bf16_f32 v33, v102, v103\n" \
  "ds_write_b128 %[h0o], v[30:33] offset:0\n" \
  "v_mfma_f32_32x32x8_bf16 v[104:119], v[150:151], v[184:185], v[104:119]\n" \
  "v_mul_f32_e32 v201, v133, v201\n" \
  "v_lshlrev_b32_e32 v88, 16, v40\n" \
  "v_and_b32_e32 v89, s64, v40\n" \
  "v_lshlrev_b32_e32 v90, 16, v41\n" \
  "v_and_b32_e32 v91, s64, v41\n" \
  "v_lshlrev_b32_e32 v92, 16, v42\n" \
  "v_and_b32_e32 v93, s64, v42\n" \
  "v_lshlrev_b32_e32 v94, 16, v43\n" \
  "v_and_b32_e32 v95, s64, v43\n" \
  "v_lshlrev_b32_e32 v96, 16, v44\n" \
  "v_and_b32_e32 v97, s64, v44\n" \
  "v_lshlrev_b32_e32 v98, 16, v45\n" \
  "v_and_b32_e32 v99, s64, v45\n" \
  "v_lshlrev_b32_e32 v100, 16, v46\n" \
  "v_and_b32_e32 v101, s64, v46\n" \
  "v_lshlrev_b32_e32 v102, 16, v47\n" \
  "v_and_b32_e32 v103, s64, v47\n" \
  "v_mul_f32_e32 v194, v251, v194\n" \
  "v_mul_f32_e32 v195, v251, v195\n" \
  "v_mfma_f32_32x32x16_bf16 v[88:103], v[72:75], v[176:179], v[88:103]\n" \
  "v_mul_f32_e32 v202, v134, v202\n" \
  "v_cvt_pk_bf16_f32 v30, v104, v105\n" \
  "v_cvt_pk_bf16_f32 v31, v106, v107\n" \
  "v_cvt_pk_bf16_f32 v32, v108, v109\n" \
  "v_cvt_pk_bf16_f32 v33, v110, v111\n" \
  "v_mfma_f32_32x32x16_bf16 v[88:103], v[76:79], v[180:183], v[88:103]\n" \
  "ds_write_b128 %[h0e], v[30:33] offset:64\n" \
  "v_cvt_pk_bf16_f32 v30, v112, v113\n" \
  "v_cvt_pk_bf16_f32 v31, v114, v115\n" \
  "v_cvt_pk_bf16_f32 v32, v116, v117\n" \
  "v_cvt_pk_bf16_f32 v33, v118, v119\n" \
  "ds_write_b128 %[h0o], v[30:33] offset:64\n" \
  "v_mfma_f32_32x32x16_bf16 v[88:103], v[24:27], v[220:223], v[88:103]\n" \
  "v_mul_f32_e32 v203, v135, v203\n" \
  "v_cvt_pk_bf16_f32 v30, v188, v189\n" \
  "v_cvt_pk_bf16_f32 v31, v190, v191\n" \
  "v_cvt_pk_bf16_f32 v32, v192, v193\n" \
  "v_cvt_pk_bf16_f32 v33, v194, v195\n" \
  "v_mfma_f32_32x32x8_bf16 v[88:103], v[28:29], v[184:185], v[88:103]\n" \
  "v_mul_f32_e32 v204, v152, v204\n" \
  "v_lshlrev_b32_e32 v104, 16, v48\n" \
  "v_and_b32_e32 v105, s64, v48\n" \
  "v_lshlrev_b32_e32 v106, 16, v49\n" \
  "v_and_b32_e32 v107, s64, v49\n" \
  "v_lshlrev_b32_e32 v108, 16, v50\n" \
  "v_and_b32_e32 v109, s64, v50\n" \
  "v_lshlrev_b32_e32 v110, 16, v51\n" \
  "v_and_b32_e32 v111, s64, v51\n" \
  "v_lshlrev_b32_e32 v112, 16, v52\n" \
  "v_and_b32_e32 v113, s64, v52\n" \
  "v_lshlrev_b32_e32 v114, 16, v53\n" \
  "v_and_b32_e32 v115, s64, v53\n" \
  "v_lshlrev_b32_e32 v116, 16, v54\n" \
  "v_and_b32_e32 v117, s64, v54\n" \
  "v_lshlrev_b32_e32 v118, 16, v55\n" \
  "v_and_b32_e32 v119, s64, v55\n" \
  "v_mul_f32_e32 v196, v251, v196\n" \
  "v_mul_f32_e32 v197, v251, v197\n" \
  "v_mfma_f32_32x32x16_bf16 v[104:119], v[136:139], v[30:33], v[104:119]\n" \
  "v_mul_f32_e32 v205, v153, v205\n" \
  "v_mul_f32_e32 v198, v251, v198\n" \
  "v_mul_f32_e32 v199, v251, v199\n" \
  "v_mul_f32_e32 v200, v251, v200\n" \
  "v_mul_f32_e32 v201, v251, v201\n" \
  "v_mul_f32_e32 v202, v251, v202\n" \
  "v_mul_f32_e32 v203, v251, v203\n" \
  "v_cvt_pk_bf16_f32 v34, v196, v197\n" \
  "v_cvt_pk_bf16_f32 v35, v198, v199\n" \
  "v_cvt_pk_bf16_f32 v36, v200, v201\n" \
  "v_cvt_pk_bf16_f32 v37, v202, v203\n" \
  "v_mul_f32_e32 v206, v154, v206\n" \
  "v_mul_f32_e32 v207, v155, v207\n" \
  "v_mfma_f32_32x32x16_bf16 v[104:119], v[140:143], v[34:37], v[104:119]\n" \
  "v_mul_f32_e32 v208, v156, v208\n" \
  "v_mul_f32_e32 v209, v157, v209\n" \
  "v_mul_f32_e32 v210, v158, v210\n" \
  "v_mul_f32_e32 v211, v159, v211\n" \
  "v_mul_f32_e32 v204, v251, v204\n" \
  "v_mul_f32_e32 v205, v251, v205\n" \
  "v_mul_f32_e32 v206, v251, v206\n" \
  "v_mul_f32_e32 v207, v251, v207\n" \
  "v_mul_f32_e32 v208, v251, v208\n" \
  "v_mul_f32_e32 v209, v251, v209\n" \
  "v_mul_f32_e32 v210, v251, v210\n" \
  "v_mul_f32_e32 v211, v251, v211\n" \
  "v_cvt_pk_bf16_f32 v38, v204, v205\n" \
  "v_cvt_pk_bf16_f32 v39, v206, v207\n" \
  "v_cvt_pk_bf16_f32 v40, v208, v209\n" \
  "v_cvt_pk_bf16_f32 v41, v210, v211\n" \
  "v_mul_f32_e32 v212, v253, v212\n" \
  "v_mul_f32_e32 v212, v251, v212\n" \
  "v_mfma_f32_32x32x16_bf16 v[104:119], v[144:147], v[38:41], v[104:119]\n" \
  "v_cvt_pk_bf16_f32 v42, v212, 0\n" \
  "v_mov_b32_e32 v43, 0\n" \
  "v_cvt_pk_bf16_f32 v44, v88, v89\n" \
  "v_cvt_pk_bf16_f32 v45, v90, v91\n" \
  "v_cvt_pk_bf16_f32 v46, v92, v93\n" \
  "v_mfma_f32_32x32x8_bf16 v[104:119], v[148:149], v[42:43], v[104:119]\n" \
  "v_cvt_pk_bf16_f32 v47, v94, v95\n" \
  "ds_write_b128 %[h0e], v[44:47] offset:128\n" \
  "v_cvt_pk_bf16_f32 v44, v96, v97\n" \
  "v_cvt_pk_bf16_f32 v45, v98, v99\n" \
  "v_cvt_pk_bf16_f32 v46, v100, v101\n" \
  "v_cvt_pk_bf16_f32 v47, v102, v103\n" \
  "ds_write_b128 %[h0o], v[44:47] offset:128\n" \
  "ds_read_b128 v[44:47], %[lin] offset:4096\n" \
  "ds_read_b128 v[48:51], %[lin] offset:5120\n" \
  "ds_read_b128 v[52:55], %[lin] offset:0\n" \
  "ds_read_b128 v[80:83], %[lin] offset:1024\n" \
  "ds_read_b128 v[84:87], %[lin] offset:2048\n" \
  "ds_read_b128 v[120:123], %[lin] offset:3072\n" \
  "s_waitcnt lgkmcnt(8)\n" \
  "ds_read_b32 v253, %[tab] offset:1024\n" \
  "ds_read_b32 v252, %[tab] offset:1280\n" \
  "ds_read_b32 v251, %[tab] offset:0\n" \
  "ds_read_b32 v250, %[tab] offset:256\n" \
  "ds_read_b32 v249, %[tab] offset:512\n" \
  "ds_read_b32 v248, %[tab] offset:768\n" \
  "v_lshlrev_b32_e32 v88, 16, v56\n" \
  "v_and_b32_e32 v89, s64, v56\n" \
  "v_lshlrev_b32_e32 v90, 16, v57\n" \
  "v_and_b32_e32 v91, s64, v57\n" \
  "v_lshlrev_b32_e32 v92, 16, v58\n" \
  "s_waitcnt lgkmcnt(10)\n" \
  "s_mov_b32 m0, s68\n" \
  "s_cbranch_scc0 Ldma4_%=\n" \
  "global_load_lds_dwordx4 v255, %[xsrc] offset:0\n" \
  "Ldma4_%=:\n" \
  "s_mov_b32 m0, s68\n" \
  "s_cbranch_scc0 Ldma5_%=\n" \
  "global_load_lds_dwordx4 v254, %[xsrc] offset:1024\n" \
  "Ldma5_%=:\n" \
  "s_waitcnt lgkmcnt(4)\n" \
  "buffer_store_dwordx4 v[44:47], v253, %[orsrc], %[ocol] offen offset:0 sc1\n" \
  "buffer_store_dwordx4 v[48:51], v252, %[orsrc], %[ocol] offen offset:1024 sc1\n" \
  "s_waitcnt lgkmcnt(2)\n" \
  "buffer_store_dwordx4 v[52:55], v251, %[orsrc], %[ocol] offen offset:0 sc1\n" \
  "buffer_store_dwordx4 v[80:83], v250, %[orsrc], %[ocol] offen offset:1024 sc1\n" \
  "s_waitcnt lgkmcnt(1)\n" \
  "buffer_store_dwordx4 v[84:87], v249, %[orsrc], %[ocol] offen offset:2048 sc1\n" \
  "s_waitcnt lgkmcnt(0)\n" \
  "buffer_store_dwordx4 v[120:123], v248, %[orsrc], %[ocol] offen offset:3072 sc1\n" \
  "v_and_b32_e32 v93, s64, v58\n" \
  "v_lshlrev_b32_e32 v94, 16, v59\n" \
  "v_and_b32_e32 v95, s64, v59\n" \
  "v_lshlrev_b32_e32 v96, 16, v60\n" \
  "v_and_b32_e32 v97, s64, v60\n" \
  "v_lshlrev_b32_e32 v98, 16, v61\n" \
  "v_and_b32_e32 v99, s64, v61\n" \
  "v_lshlrev_b32_e32 v100, 16, v62\n" \
  "v_and_b32_e32 v101, s64, v62\n" \
  "v_lshlrev_b32_e32 v102, 16, v63\n" \
  "v_and_b32_e32 v103, s64, v63\n" \
  "v_cvt_pk_bf16_f32 v44, v104, v105\n" \
  "v_cvt_pk_bf16_f32 v45, v106, v107\n" \
  "v_mfma_f32_32x32x16_bf16 v[88:103], v[160:163], v[30:33], v[88:103]\n" \
  "v_cvt_pk_bf16_f32 v46, v108, v109\n" \
  "v_cvt_pk_bf16_f32 v47, v110, v111\n" \
  "ds_write_b128 %[h1e], v[44:47] offset:0\n" \
  "ds_read_b32 v255, %[tab] offset:0\n" \
  "ds_read_b32 v254, %[tab] offset:256\n" \
  "v_mfma_f32_32x32x16_bf16 v[88:103], v[164:167], v[34:37], v[88:103]\n" \
  "ds_read_b32 v253, %[tab] offset:512\n" \
  "ds_read_b32 v252, %[tab] offset:768\n" \
  "v_cvt_pk_bf16_f32 v44, v112, v113\n" \
  "v_cvt_pk_bf16_f32 v45, v114, v115\n" \
  "v_cvt_pk_bf16_f32 v46, v116, v117\n" \
  "v_cvt_pk_bf16_f32 v47, v118, v119\n" \
  "v_mfma_f32_32x32x16_bf16 v[88:103], v[168:171], v[38:41], v[88:103]\n" \
  "ds_write_b128 %[h1o], v[44:47] offset:0\n" \
  "v_lshlrev_b32_e32 v104, 16, v64\n" \
  "v_and_b32_e32 v105, s64, v64\n" \
  "v_lshlrev_b32_e32 v106, 16, v65\n" \
  "v_and_b32_e32 v107, s64, v65\n" \
  "v_lshlrev_b32_e32 v108, 16, v66\n" \
  "v_and_b32_e32 v109, s64, v66\n" \
  "v_mfma_f32_32x32x8_bf16 v[88:103], v[150:151], v[42:43], v[88:103]\n" \
  "v_lshlrev_b32_e32 v110, 16, v67\n" \
  "v_and_b32_e32 v111, s64, v67\n" \
  "v_lshlrev_b32_e32 v112, 16, v68\n" \
  "v_and_b32_e32 v113, s64, v68\n" \
  "v_lshlrev_b32_e32 v114, 16, v69\n" \
  "v_and_b32_e32 v115, s64, v69\n" \
  "v_lshlrev_b32_e32 v116, 16, v70\n" \
  "v_and_b32_e32 v117, s64, v70\n" \
  "v_lshlrev_b32_e32 v118, 16, v71\n" \
  "v_and_b32_e32 v119, s64, v71\n" \
  "s_nop 1\n" \
  "v_mfma_f32_32x32x16_bf16 v[104:119], v[72:75], v[30:33], v[104:119]\n" \
  "v_cvt_pk_bf16_f32 v30, v88, v89\n" \
  "v_cvt_pk_bf16_f32 v31, v90, v91\n" \
  "v_cvt_pk_bf16_f32 v32, v92, v93\n" \
  "v_cvt_pk_bf16_f32 v33, v94, v95\n" \
  "ds_write_b128 %[h1e], v[30:33] offset:64\n" \
  "v_mfma_f32_32x32x16_bf16 v[104:119], v[76:79], v[34:37], v[104:119]\n" \
  "v_cvt_pk_bf16_f32 v30, v96, v97\n" \
  "v_cvt_pk_bf16_f32 v31, v98, v99\n" \
  "v_cvt_pk_bf16_f32 v32, v100, v101\n" \
  "v_cvt_pk_bf16_f32 v33, v102, v103\n" \
  "ds_write_b128 %[h1o], v[30:33] offset:64\n" \
  "v_mfma_f32_32x32x16_bf16 v[104:119], v[24:27], v[38:41], v[104:119]\n" \
  "v_mfma_f32_32x32x8_bf16 v[104:119], v[28:29], v[42:43], v[104:119]\n" \
  "s_nop 11\n" \
  "v_cvt_pk_bf16_f32 v24, v104, v105\n" \
  "v_cvt_pk_bf16_f32 v25, v106, v107\n" \
  "v_cvt_pk_bf16_f32 v26, v108, v109\n" \
  "v_cvt_pk_bf16_f32 v27, v110, v111\n" \
  "ds_write_b128 %[h1e], v[24:27] offset:128\n" \
  "v_cvt_pk_bf16_f32 v24, v112, v113\n" \
  "v_cvt_pk_bf16_f32 v25, v114, v115\n" \
  "v_cvt_pk_bf16_f32 v26, v116, v117\n" \
  "v_cvt_pk_bf16_f32 v27, v118, v119\n" \
  "ds_write_b128 %[h1o], v[24:27] offset:128\n" \
  "ds_read_b128 v[24:27], %[lin] offset:0\n" \
  "ds_read_b128 v[28:31], %[lin] offset:1024\n" \
  "ds_read_b128 v[32:35], %[lin] offset:2048\n" \
  "ds_read_b128 v[36:39], %[lin] offset:3072\n" \
  "ds_read_b32 v251, %[tab] offset:1536\n" \
  "s_waitcnt lgkmcnt(7)\n" \
  "ds_read_b32 v250, %[tab] offset:1792\n" \
  "ds_read_b32 v249, %[tab] offset:2048\n" \
  "ds_read_b32 v248, %[tab] offset:2304\n" \
  "s_waitcnt lgkmcnt(4)\n" \
  "s_mov_b32 m0, %[xdst]\n" \
  "s_cbranch_scc0 Ldma0_%=\n" \
  "global_load_lds_dwordx4 v255, %[xsrc] offset:0\n" \
  "Ldma0_%=:\n" \
  "s_mov_b32 m0, %[xdst]\n" \
  "s_cbranch_scc0 Ldma1_%=\n" \
  "global_load_lds_dwordx4 v254, %[xsrc] offset:1024\n" \
  "Ldma1_%=:\n" \
  "s_mov_b32 m0, %[xdst]\n" \
  "s_cbranch_scc0 Ldma2_%=\n" \
  "global_load_lds_dwordx4 v253, %[xsrc] offset:2048\n" \
  "Ldma2_%=:\n" \
  "s_mov_b32 m0, %[xdst]\n" \
  "s_cbranch_scc0 Ldma3_%=\n" \
  "global_load_lds_dwordx4 v252, %[xsrc] offset:3072\n" \
  "Ldma3_%=:\n" \
  "s_waitcnt lgkmcnt(0)\n" \
  "buffer_store_dwordx4 v[24:27], v251, %[orsrc], %[ocol] offen offset:2048 sc1\n" \
  "buffer_store_dwordx4 v[28:31], v250, %[orsrc], %[ocol] offen offset:3072 sc1\n" \
  "buffer_store_dwordx4 v[32:35], v249, %[orsrc], %[ocol] offen offset:0 sc1\n" \
  "buffer_store_dwordx4 v[36:39], v248, %[orsrc], %[ocol] offen offset:1024 sc1\n" \
  "s_mov_b32 m0, s70\n"

#define PA_BLOCK_ASM \
  "s_waitcnt lgkmcnt(0)\n" \
  "ds_read_b128 v[48:51], %[b02] offset:128\n" \
  "ds_read_b128 v[52:55], %[b00] offset:0\n" \
  "ds_read_b128 v[56:59], %[b03] offset:128\n" \
  "ds_read_b128 v[60:63], %[b01] offset:0\n" \
  "ds_read_b128 v[64:67], %[b00] offset:256\n" \
  "ds_read_b128 v[68:71], %[b02] offset:0\n" \
  "ds_read_b128 v[72:75], %[b01] offset:256\n" \
  "ds_read_b128 v[76:79], %[b03] offset:0\n" \
  "ds_read_b128 v[80:83], %[b02] offset:256\n" \
  "ds_read_b128 v[84:87], %[b00] offset:128\n" \
  "ds_read_b128 v[88:91], %[b03] offset:256\n" \
  "ds_read_b128 v[92:95], %[b01] offset:128\n" \
  "ds_read_b128 v[96:99], %[b12] offset:128\n" \
  "ds_read_b128 v[100:103], %[b13] offset:128\n" \
  "ds_read_b128 v[104:107], %[b10] offset:256\n" \
  "s_waitcnt lgkmcnt(13)\n" \
  "ds_read_b128 v[108:111], %[b11] offset:256\n" \
  "v_mfma_f32_32x32x16_bf16 v[112:127], v[48:51], v[52:55], 0\n" \
  "ds_read_b128 v[128:131], %[b12] offset:256\n" \
  "s_waitcnt lgkmcnt(13)\n" \
  "ds_read_b128 v[132:135], %[b13] offset:256\n" \
  "ds_read_b128 v[136:139], %[b22] offset:128\n" \
  "s_waitcnt lgkmcnt(13)\n" \
  "ds_read_b128 v[140:143], %[b23] offset:128\n" \
  "ds_read_b128 v[144:147], %[b20] offset:256\n" \
  "s_waitcnt lgkmcnt(13)\n" \
  "ds_read_b128 v[148:151], %[b21] offset:256\n" \
  "v_mfma_f32_32x32x16_bf16 v[112:127], v[56:59], v[60:63], v[112:127]\n" \
  "ds_read_b128 v[152:155], %[b22] offset:256\n" \
  "s_waitcnt lgkmcnt(13)\n" \
  "ds_read_b128 v[156:159], %[b23] offset:256\n" \
  "ds_read_b128 v[160:163], %[b10] offset:0\n" \
  "s_waitcnt lgkmcnt(13)\n" \
  "ds_read_b128 v[164:167], %[b11] offset:0\n" \
  "ds_read_b128 v[168:171], %[b12] offset:0\n" \
  "s_waitcnt lgkmcnt(13)\n" \
  "ds_read_b128 v[172:175], %[b13] offset:0\n" \
  "v_mfma_f32_32x32x16_bf16 v[112:127], v[64:67], v[68:71], v[112:127]\n" \
  "ds_read_b128 v[176:179], %[b10] offset:128\n" \
  "s_waitcnt lgkmcnt(13)\n" \
  "ds_read_b128 v[180:183], %[b11] offset:128\n" \
  "ds_read_b128 v[184:187], %[b20] offset:0\n" \
  "s_waitcnt lgkmcnt(13)\n" \
  "ds_read_b128 v[188:191], %[b21] offset:0\n" \
  "ds_read_b128 v[192:195], %[b22] offset:0\n" \
  "s_waitcnt lgkmcnt(13)\n" \
  "ds_read_b128 v[196:199], %[b23] offset:0\n" \
  "v_mfma_f32_32x32x16_bf16 v[112:127], v[72:75], v[76:79], v[112:127]\n" \
  "ds_read_b128 v[200:203], %[b20] offset:128\n" \
  "s_waitcnt lgkmcnt(13)\n" \
  "ds_read_b128 v[204:207], %[b21] offset:128\n" \
  "v_mov_b32_e32 v255, 1\n" \
  "v_mov_b32_e32 v254, 0xbc2aaaab\n" \
  "v_mov_b32_e32 v253, 0x3e16c740\n" \
  "v_mfma_f32_32x32x16_bf16 v[112:127], v[80:83], v[84:87], v[112:127]\n" \
  "v_mfma_f32_32x32x16_bf16 v[112:127], v[88:91], v[92:95], v[112:127]\n" \
  "s_waitcnt lgkmcnt(0)\n" \
  "s_mov_b64 s[72:73], exec\n" \
  "s_mov_b64 exec, 1\n" \
  "ds_add_u32 %[cnt], v255 offset:0\n" \
  "s_mov_b64 exec, s[72:73]\n" \
  "v_mfma_f32_32x32x16_bf16 v[112:127], v[96:99], v[52:55], v[112:127]\n" \
  "v_mfma_f32_32x32x16_bf16 v[112:127], v[100:103], v[60:63], v[112:127]\n" \
  "v_mfma_f32_32x32x16_bf16 v[112:127], v[104:107], v[68:71], v[112:127]\n" \
  "v_mfma_f32_32x32x16_bf16 v[112:127], v[108:111], v[76:79], v[112:127]\n" \
  "v_mfma_f32_32x32x16_bf16 v[112:127], v[128:131], v[84:87], v[112:127]\n" \
  "v_mfma_f32_32x32x16_bf16 v[112:127], v[132:135], v[92:95], v[112:127]\n" \
  "v_mfma_f32_32x32x16_bf16 v[112:127], v[136:139], v[52:55], v[112:127]\n" \
  "v_mfma_f32_32x32x16_bf16 v[112:127], v[140:143], v[60:63], v[112:127]\n" \
  "v_mfma_f32_32x32x16_bf16 v[112:127], v[144:147], v[68:71], v[112:127]\n" \
  "v_mfma_f32_32x32x16_bf16 v[112:127], v[148:151], v[76:79], v[112:127]\n" \
  "v_mfma_f32_32x32x16_bf16 v[112:127], v[152:155], v[84:87], v[112:127]\n" \
  "v_mfma_f32_32x32x16_bf16 v[112:127], v[156:159], v[92:95], v[112:127]\n" \
  "v_mfma_f32_32x32x16_bf16 v[96:111], v[52:55], v[48:51], 0\n" \
  "s_nop 10\n" \
  "v_add_f32_e32 v252, v112, v113\n" \
  "v_mfma_f32_32x32x16_bf16 v[96:111], v[60:63], v[56:59], v[96:111]\n" \
  "v_add_f32_e32 v251, v114, v115\n" \
  "v_add_f32_e32 v252, v252, v116\n" \
  "v_add_f32_e32 v251, v251, v117\n" \
  "v_add_f32_e32 v252, v252, v118\n" \
  "v_add_f32_e32 v251, v251, v119\n" \
  "v_add_f32_e32 v252, v252, v120\n" \
  "v_mfma_f32_32x32x16_bf16 v[96:111], v[68:71], v[64:67], v[96:111]\n" \
  "v_add_f32_e32 v251, v251, v121\n" \
  "v_add_f32_e32 v252, v252, v122\n" \
  "v_add_f32_e32 v251, v251, v123\n" \
  "v_add_f32_e32 v252, v252, v124\n" \
  "v_add_f32_e32 v251, v251, v125\n" \
  "v_add_f32_e32 v252, v252, v126\n" \
  "v_mfma_f32_32x32x16_bf16 v[96:111], v[76:79], v[72:75], v[96:111]\n" \
  "v_add_f32_e32 v251, v251, v127\n" \
  "v_add_f32_e32 v252, v252, v251\n" \
  "v_mov_b32_e32 v251, v252\n" \
  "s_nop 1\n" \
  "v_permlane32_swap_b32_e32 v252, v251\n" \
  "v_mfma_f32_32x32x16_bf16 v[96:111], v[84:87], v[80:83], v[96:111]\n" \
  "v_add_f32_e32 v252, v252, v251\n" \
  "ds_write_b32 %[rhow], v252 offset:0\n" \
  "s_mov_b64 s[72:73], exec\n" \
  "s_mov_b64 exec, 1\n" \
  "ds_add_u32 %[cnt], v255 offset:4\n" \
  "s_mov_b64 exec, s[72:73]\n" \
  "v_mfma_f32_32x32x16_bf16 v[96:111], v[92:95], v[88:91], v[96:111]\n" \
  "v_mfma_f32_32x32x16_bf16 v[112:127], v[160:163], v[48:51], 0\n" \
  "v_mfma_f32_32x32x16_bf16 v[112:127], v[164:167], v[56:59], v[112:127]\n" \
  "v_mfma_f32_32x32x16_bf16 v[112:127], v[168:171], v[64:67], v[112:127]\n" \
  "v_mfma_f32_32x32x16_bf16 v[112:127], v[172:175], v[72:75], v[112:127]\n" \
  "v_mfma_f32_32x32x16_bf16 v[112:127], v[176:179], v[80:83], v[112:127]\n" \
  "v_mfma_f32_32x32x16_bf16 v[112:127], v[180:183], v[88:91], v[112:127]\n" \
  "s_movk_i32 s75, 0x7fff\n" \
  "Lspin94PA_%=:\n" \
  "ds_read_b32 v255, %[cnt] offset:4\n" \
  "s_waitcnt lgkmcnt(0)\n" \
  "v_readfirstlane_b32 s74, v255\n" \
  "s_sub_u32 s75, s75, 1\n" \
  "s_cbranch_scc1 Lspin94PA_%=x\n" \
  "s_cmp_ge_u32 s74, 3\n" \
  "s_cbranch_scc1 Lspin94PA_%=x\n" \
  "s_sleep 1\n" \
  "s_branch Lspin94PA_%=\n" \
  "Lspin94PA_%=x:\n" \
  "ds_read_b128 v[52:55], %[rhor0] offset:0\n" \
  "ds_read_b128 v[60:63], %[rhor0] offset:16\n" \
  "ds_read_b128 v[68:71], %[rhor0] offset:64\n" \
  "ds_read_b128 v[76:79], %[rhor0] offset:80\n" \
  "ds_read_b128 v[84:87], %[rhor1] offset:0\n" \
  "v_mfma_f32_32x32x16_bf16 v[128:143], v[184:187], v[48:51], 0\n" \
  "ds_read_b128 v[48:51], %[rhor1] offset:16\n" \
  "ds_read_b128 v[92:95], %[rhor1] offset:64\n" \
  "ds_read_b128 v[144:147], %[rhor1] offset:80\n" \
  "ds_read_b128 v[148:151], %[rhor2] offset:0\n" \
  "ds_read_b128 v[152:155], %[rhor2] offset:16\n" \
  "ds_read_b128 v[156:159], %[rhor2] offset:64\n" \
  "v_mfma_f32_32x32x16_bf16 v[128:143], v[188:191], v[56:59], v[128:143]\n" \
  "ds_read_b128 v[56:59], %[rhor2] offset:80\n" \
  "s_waitcnt lgkmcnt(11)\n" \
  "v_fmac_f32_e32 v96, v52, v254\n" \
  "v_fmac_f32_e32 v97, v53, v254\n" \
  "v_fmac_f32_e32 v98, v54, v254\n" \
  "v_fmac_f32_e32 v99, v55, v254\n" \
  "s_waitcnt lgkmcnt(10)\n" \
  "v_fmac_f32_e32 v100, v60, v254\n" \
  "v_mfma_f32_32x32x16_bf16 v[128:143], v[192:195], v[64:67], v[128:143]\n" \
  "v_fmac_f32_e32 v101, v61, v254\n" \
  "v_add_f32_e32 v255, v96, v97\n" \
  "v_add_f32_e32 v252, v98, v99\n" \
  "v_fmac_f32_e32 v102, v62, v254\n" \
  "v_fmac_f32_e32 v103, v63, v254\n" \
  "v_add_f32_e32 v255, v255, v100\n" \
  "v_mfma_f32_32x32x16_bf16 v[128:143], v[196:199], v[72:75], v[128:143]\n" \
  "s_waitcnt lgkmcnt(4)\n" \
  "v_fmac_f32_e32 v104, v68, v254\n" \
  "v_add_f32_e32 v252, v252, v101\n" \
  "v_fmac_f32_e32 v105, v69, v254\n" \
  "v_add_f32_e32 v255, v255, v102\n" \
  "v_add_f32_e32 v252, v252, v103\n" \
  "v_fmac_f32_e32 v106, v70, v254\n" \
  "v_mfma_f32_32x32x16_bf16 v[128:143], v[200:203], v[80:83], v[128:143]\n" \
  "v_fmac_f32_e32 v107, v71, v254\n" \
  "v_add_f32_e32 v255, v255, v104\n" \
  "v_add_f32_e32 v252, v252, v105\n" \
  "v_fmac_f32_e32 v108, v76, v254\n" \
  "v_fmac_f32_e32 v109, v77, v254\n" \
  "v_add_f32_e32 v255, v255, v106\n" \
  "v_mfma_f32_32x32x16_bf16 v[128:143], v[204:207], v[88:91], v[128:143]\n" \
  "v_fmac_f32_e32 v110, v78, v254\n" \
  "v_add_f32_e32 v252, v252, v107\n" \
  "v_fmac_f32_e32 v111, v79, v254\n" \
  "v_add_f32_e32 v255, v255, v108\n" \
  "v_add_f32_e32 v252, v252, v109\n" \
  "v_fmac_f32_e32 v112, v84, v254\n" \
  "v_fmac_f32_e32 v113, v85, v254\n" \
  "v_add_f32_e32 v255, v255, v110\n" \
  "v_add_f32_e32 v252, v252, v111\n" \
  "v_fmac_f32_e32 v114, v86, v254\n" \
  "v_fmac_f32_e32 v115, v87, v254\n" \
  "v_add_f32_e32 v255, v255, v112\n" \
  "v_add_f32_e32 v252, v252, v113\n" \
  "v_fmac_f32_e32 v116, v48, v254\n" \
  "v_fmac_f32_e32 v117, v49, v254\n" \
  "v_add_f32_e32 v255, v255, v114\n" \
  "v_add_f32_e32 v252, v252, v115\n" \
  "v_fmac_f32_e32 v118, v50, v254\n" \
  "v_fmac_f32_e32 v119, v51, v254\n" \
  "v_add_f32_e32 v255, v255, v116\n" \
  "v_add_f32_e32 v252, v252, v117\n" \
  "v_fmac_f32_e32 v120, v92, v254\n" \
  "v_fmac_f32_e32 v121, v93, v254\n" \
  "v_add_f32_e32 v255, v255, v118\n" \
  "v_add_f32_e32 v252, v252, v119\n" \
  "v_fmac_f32_e32 v122, v94, v254\n" \
  "v_fmac_f32_e32 v123, v95, v254\n" \
  "v_add_f32_e32 v255, v255, v120\n" \
  "v_add_f32_e32 v252, v252, v121\n" \
  "v_fmac_f32_e32 v124, v144, v254\n" \
  "v_fmac_f32_e32 v125, v145, v254\n" \
  "v_add_f32_e32 v255, v255, v122\n" \
  "v_add_f32_e32 v252, v252, v123\n" \
  "v_fmac_f32_e32 v126, v146, v254\n" \
  "v_fmac_f32_e32 v127, v147, v254\n" \
  "v_add_f32_e32 v255, v255, v124\n" \
  "v_add_f32_e32 v252, v252, v125\n" \
  "s_waitcnt lgkmcnt(0)\n" \
  "v_fmac_f32_e32 v128, v148, v254\n" \
  "v_fmac_f32_e32 v129, v149, v254\n" \
  "v_add_f32_e32 v255, v255, v126\n" \
  "v_add_f32_e32 v252, v252, v127\n" \
  "v_fmac_f32_e32 v130, v150, v254\n" \
  "v_fmac_f32_e32 v131, v151, v254\n" \
  "v_add_f32_e32 v255, v255, v128\n" \
  "v_add_f32_e32 v252, v252, v129\n" \
  "v_fmac_f32_e32 v132, v152, v254\n" \
  "v_fmac_f32_e32 v133, v153, v254\n" \
  "v_add_f32_e32 v255, v255, v130\n" \
  "v_add_f32_e32 v252, v252, v131\n" \
  "v_fmac_f32_e32 v134, v154, v254\n" \
  "v_fmac_f32_e32 v135, v155, v254\n" \
  "v_add_f32_e32 v255, v255, v132\n" \
  "v_add_f32_e32 v252, v252, v133\n" \
  "v_fmac_f32_e32 v136, v156, v254\n" \
  "v_fmac_f32_e32 v137, v157, v254\n" \
  "v_add_f32_e32 v255, v255, v134\n" \
  "v_add_f32_e32 v252, v252, v135\n" \
  "v_fmac_f32_e32 v138, v158, v254\n" \
  "v_fmac_f32_e32 v139, v159, v254\n" \
  "v_add_f32_e32 v255, v255, v136\n" \
  "v_add_f32_e32 v252, v252, v137\n" \
  "v_fmac_f32_e32 v140, v56, v254\n" \
  "v_fmac_f32_e32 v141, v57, v254\n" \
  "v_add_f32_e32 v255, v255, v138\n" \
  "v_add_f32_e32 v252, v252, v139\n" \
  "v_fmac_f32_e32 v142, v58, v254\n" \
  "v_fmac_f32_e32 v143, v59, v254\n" \
  "v_add_f32_e32 v255, v255, v140\n" \
  "v_add_f32_e32 v252, v252, v141\n" \
  "v_add_f32_e32 v255, v255, v142\n" \
  "v_add_f32_e32 v252, v252, v143\n" \
  "v_add_f32_e32 v255, v255, v252\n" \
  "v_mov_b32_e32 v254, v255\n" \
  "s_nop 1\n" \
  "v_permlane32_swap_b32_e32 v255, v254\n" \
  "v_add_f32_e32 v255, v255, v254\n" \
  "v_mul_f32_e32 v254, 0xbac909aa, v255\n" \
  "v_fma_f32 v96, v96, v253, v254\n" \
  "v_fma_f32 v97, v97, v253, v254\n" \
  "v_fma_f32 v98, v98, v253, v254\n" \
  "v_fma_f32 v99, v99, v253, v254\n" \
  "v_fma_f32 v100, v100, v253, v254\n" \
  "v_fma_f32 v101, v101, v253, v254\n" \
  "v_fma_f32 v102, v102, v253, v254\n" \
  "v_fma_f32 v103, v103, v253, v254\n" \
  "v_fma_f32 v104, v104, v253, v254\n" \
  "v_fma_f32 v105, v105, v253, v254\n" \
  "v_fma_f32 v106, v106, v253, v254\n" \
  "v_fma_f32 v107, v107, v253, v254\n" \
  "v_fma_f32 v108, v108, v253, v254\n" \
  "v_fma_f32 v109, v109, v253, v254\n" \
  "v_fma_f32 v110, v110, v253, v254\n" \
  "v_fma_f32 v111, v111, v253, v254\n" \
  "v_fma_f32 v112, v112, v253, v254\n" \
  "v_fma_f32 v113, v113, v253, v254\n" \
  "v_fma_f32 v114, v114, v253, v254\n" \
  "v_fma_f32 v115, v115, v253, v254\n" \
  "v_fma_f32 v116, v116, v253, v254\n" \
  "v_fma_f32 v117, v117, v253, v254\n" \
  "v_fma_f32 v118, v118, v253, v254\n" \
  "v_fma_f32 v119, v119, v253, v254\n" \
  "v_fma_f32 v120, v120, v253, v254\n" \
  "v_fma_f32 v121, v121, v253, v254\n" \
  "v_fma_f32 v122, v122, v253, v254\n" \
  "v_fma_f32 v123, v123, v253, v254\n" \
  "v_fma_f32 v124, v124, v253, v254\n" \
  "v_fma_f32 v125, v125, v253, v254\n" \
  "v_fma_f32 v126, v126, v253, v254\n" \
  "v_fma_f32 v127, v127, v253, v254\n" \
  "v_fma_f32 v128, v128, v253, v254\n" \
  "v_fma_f32 v129, v129, v253, v254\n" \
  "v_fma_f32 v130, v130, v253, v254\n" \
  "v_fma_f32 v131, v131, v253, v254\n" \
  "v_fma_f32 v132, v132, v253, v254\n" \
  "v_fma_f32 v133, v133, v253, v254\n" \
  "v_fma_f32 v134, v134, v253, v254\n" \
  "v_fma_f32 v135, v135, v253, v254\n" \
  "v_fma_f32 v136, v136, v253, v254\n" \
  "v_fma_f32 v137, v137, v253, v254\n" \
  "v_fma_f32 v138, v138, v253, v254\n" \
  "v_fma_f32 v139, v139, v253, v254\n" \
  "v_fma_f32 v140, v140, v253, v254\n" \
  "v_fma_f32 v141, v141, v253, v254\n" \
  "v_fma_f32 v142, v142, v253, v254\n" \
  "v_fma_f32 v143, v143, v253, v254\n" \
  "v_cvt_pk_bf16_f32 v48, v96, v97\n" \
  "v_cvt_pk_bf16_f32 v49, v98, v99\n" \
  "v_cvt_pk_bf16_f32 v50, v100, v101\n" \
  "v_cvt_pk_bf16_f32 v51, v102, v103\n" \
  "v_cvt_pk_bf16_f32 v52, v104, v105\n" \
  "v_cvt_pk_bf16_f32 v53, v106, v107\n" \
  "v_cvt_pk_bf16_f32 v54, v108, v109\n" \
  "v_cvt_pk_bf16_f32 v55, v110, v111\n" \
  "v_cvt_pk_bf16_f32 v56, v112, v113\n" \
  "v_cvt_pk_bf16_f32 v57, v114, v115\n" \
  "v_cvt_pk_bf16_f32 v58, v116, v117\n" \
  "v_cvt_pk_bf16_f32 v59, v118, v119\n" \
  "v_cvt_pk_bf16_f32 v60, v120, v121\n" \
  "v_cvt_pk_bf16_f32 v61, v122, v123\n" \
  "v_cvt_pk_bf16_f32 v62, v124, v125\n" \
  "v_cvt_pk_bf16_f32 v63, v126, v127\n" \
  "v_cvt_pk_bf16_f32 v64, v128, v129\n" \
  "v_cvt_pk_bf16_f32 v65, v130, v131\n" \
  "v_cvt_pk_bf16_f32 v66, v132, v133\n" \
  "v_cvt_pk_bf16_f32 v67, v134, v135\n" \
  "v_cvt_pk_bf16_f32 v68, v136, v137\n" \
  "v_cvt_pk_bf16_f32 v69, v138, v139\n" \
  "v_cvt_pk_bf16_f32 v70, v140, v141\n" \
  "v_cvt_pk_bf16_f32 v71, v142, v143\n" \
  "s_movk_i32 s75, 0x7fff\n" \
  "Lspin280PA_%=:\n" \
  "ds_read_b32 v255, %[cnt] offset:8\n" \
  "s_waitcnt lgkmcnt(0)\n" \
  "v_readfirstlane_b32 s74, v255\n" \
  "s_sub_u32 s75, s75, 1\n" \
  "s_cbranch_scc1 Lspin280PA_%=x\n" \
  "s_cmp_ge_u32 s74, %[tgt2]\n" \
  "s_cbranch_scc1 Lspin280PA_%=x\n" \
  "s_sleep 1\n" \
  "s_branch Lspin280PA_%=\n" \
  "Lspin280PA_%=x:\n" \
  "ds_write_b128 %[dst0], v[48:51] offset:0\n" \
  "ds_write_b128 %[dst0], v[52:55] offset:1024\n" \
  "ds_write_b128 %[dst1], v[56:59] offset:0\n" \
  "ds_write_b128 %[dst1], v[60:63] offset:1024\n" \
  "ds_write_b128 %[dst2], v[64:67] offset:0\n" \
  "ds_write_b128 %[dst2], v[68:71] offset:1024\n"

#define PB_BLOCK_ASM \
  "s_waitcnt lgkmcnt(0)\n" \
  "ds_read_b128 v[24:27], %[ve] offset:0\n" \
  "ds_read_b64_tr_b16 v[28:29], %[tb] offset:0\n" \
  "ds_read_b64_tr_b16 v[30:31], %[tb] offset:768\n" \
  "ds_read_b128 v[32:35], %[vo] offset:0\n" \
  "ds_read_b64_tr_b16 v[36:37], %[tb] offset:3072\n" \
  "ds_read_b64_tr_b16 v[38:39], %[tb] offset:3840\n" \
  "ds_read_b128 v[40:43], %[ve] offset:64\n" \
  "ds_read_b64_tr_b16 v[44:45], %[tb] offset:6144\n" \
  "ds_read_b64_tr_b16 v[46:47], %[tb] offset:6912\n" \
  "ds_read_b128 v[48:51], %[vo] offset:64\n" \
  "ds_read_b64_tr_b16 v[52:53], %[tb] offset:9216\n" \
  "ds_read_b64_tr_b16 v[54:55], %[tb] offset:9984\n" \
  "ds_read_b128 v[56:59], %[ve] offset:128\n" \
  "ds_read_b64_tr_b16 v[60:61], %[tb] offset:12288\n" \
  "ds_read_b64_tr_b16 v[62:63], %[tb] offset:13056\n" \
  "s_waitcnt lgkmcnt(13)\n" \
  "ds_read_b128 v[64:67], %[vo] offset:128\n" \
  "ds_read_b64_tr_b16 v[68:69], %[tb] offset:15360\n" \
  "s_waitcnt lgkmcnt(14)\n" \
  "v_mfma_f32_32x32x16_bf16 v[72:87], v[24:27], v[28:31], 0\n" \
  "ds_read_b64_tr_b16 v[70:71], %[tb] offset:16128\n" \
  "s_waitcnt lgkmcnt(13)\n" \
  "ds_read_b128 v[88:91], %[ve] offset:6144\n" \
  "ds_read_b128 v[92:95], %[vo] offset:6144\n" \
  "s_waitcnt lgkmcnt(13)\n" \
  "ds_read_b128 v[96:99], %[ve] offset:6208\n" \
  "ds_read_b128 v[100:103], %[vo] offset:6208\n" \
  "s_waitcnt lgkmcnt(13)\n" \
  "ds_read_b128 v[104:107], %[ve] offset:6272\n" \
  "v_mfma_f32_32x32x16_bf16 v[72:87], v[32:35], v[36:39], v[72:87]\n" \
  "ds_read_b128 v[108:111], %[vo] offset:6272\n" \
  "s_waitcnt lgkmcnt(13)\n" \
  "ds_read_b128 v[112:115], %[ve] offset:12288\n" \
  "ds_read_b128 v[116:119], %[vo] offset:12288\n" \
  "s_waitcnt lgkmcnt(13)\n" \
  "ds_read_b128 v[120:123], %[ve] offset:12352\n" \
  "ds_read_b128 v[124:127], %[vo] offset:12352\n" \
  "s_waitcnt lgkmcnt(13)\n" \
  "ds_read_b128 v[128:131], %[ve] offset:12416\n" \
  "v_mfma_f32_32x32x16_bf16 v[72:87], v[40:43], v[44:47], v[72:87]\n" \
  "ds_read_b128 v[132:135], %[vo] offset:12416\n" \
  "s_waitcnt lgkmcnt(13)\n" \
  "ds_read_b64_tr_b16 v[136:137], %[tb] offset:64\n" \
  "ds_read_b64_tr_b16 v[138:139], %[tb] offset:832\n" \
  "s_waitcnt lgkmcnt(13)\n" \
  "ds_read_b64_tr_b16 v[140:141], %[tb] offset:3136\n" \
  "ds_read_b64_tr_b16 v[142:143], %[tb] offset:3904\n" \
  "s_waitcnt lgkmcnt(13)\n" \
  "ds_read_b64_tr_b16 v[144:145], %[tb] offset:6208\n" \
  "v_mfma_f32_32x32x16_bf16 v[72:87], v[48:51], v[52:55], v[72:87]\n" \
  "ds_read_b64_tr_b16 v[146:147], %[tb] offset:6976\n" \
  "s_waitcnt lgkmcnt(13)\n" \
  "ds_read_b64_tr_b16 v[148:149], %[tb] offset:9280\n" \
  "ds_read_b64_tr_b16 v[150:151], %[tb] offset:10048\n" \
  "s_waitcnt lgkmcnt(13)\n" \
  "ds_read_b64_tr_b16 v[152:153], %[tb] offset:12352\n" \
  "ds_read_b64_tr_b16 v[154:155], %[tb] offset:13120\n" \
  "s_waitcnt lgkmcnt(13)\n" \
  "ds_read_b64_tr_b16 v[156:157], %[tb] offset:15424\n" \
  "v_mfma_f32_32x32x16_bf16 v[72:87], v[56:59], v[60:63], v[72:87]\n" \
  "ds_read_b64_tr_b16 v[158:159], %[tb] offset:16192\n" \
  "s_waitcnt lgkmcnt(13)\n" \
  "ds_read_b64_tr_b16 v[160:161], %[tb] offset:128\n" \
  "ds_read_b64_tr_b16 v[162:163], %[tb] offset:896\n" \
  "s_waitcnt lgkmcnt(13)\n" \
  "ds_read_b64_tr_b16 v[164:165], %[tb] offset:3200\n" \
  "ds_read_b64_tr_b16 v[166:167], %[tb] offset:3968\n" \
  "s_waitcnt lgkmcnt(13)\n" \
  "ds_read_b64_tr_b16 v[168:169], %[tb] offset:6272\n" \
  "v_mfma_f32_32x32x16_bf16 v[72:87], v[64:67], v[68:71], v[72:87]\n" \
  "ds_read_b64_tr_b16 v[170:171], %[tb] offset:7040\n" \
  "s_waitcnt lgkmcnt(13)\n" \
  "ds_read_b64_tr_b16 v[172:173], %[tb] offset:9344\n" \
  "ds_read_b64_tr_b16 v[174:175], %[tb] offset:10112\n" \
  "s_waitcnt lgkmcnt(13)\n" \
  "ds_read_b64_tr_b16 v[176:177], %[tb] offset:12416\n" \
  "ds_read_b64_tr_b16 v[178:179], %[tb] offset:13184\n" \
  "s_waitcnt lgkmcnt(13)\n" \
  "ds_read_b64_tr_b16 v[180:181], %[tb] offset:15488\n" \
  "v_mfma_f32_32x32x16_bf16 v[184:199], v[88:91], v[28:31], 0\n" \
  "ds_read_b64_tr_b16 v[182:183], %[tb] offset:16256\n" \
  "v_mov_b32_e32 v255, 1\n" \
  "v_add_f32_e32 v254, v72, v73\n" \
  "v_add_f32_e32 v253, v74, v75\n" \
  "v_mfma_f32_32x32x16_bf16 v[184:199], v[92:95], v[36:39], v[184:199]\n" \
  "v_add_f32_e32 v254, v254, v76\n" \
  "v_add_f32_e32 v253, v253, v77\n" \
  "v_add_f32_e32 v254, v254, v78\n" \
  "v_add_f32_e32 v253, v253, v79\n" \
  "v_add_f32_e32 v254, v254, v80\n" \
  "v_add_f32_e32 v253, v253, v81\n" \
  "v_mfma_f32_32x32x16_bf16 v[184:199], v[96:99], v[44:47], v[184:199]\n" \
  "v_add_f32_e32 v254, v254, v82\n" \
  "s_waitcnt lgkmcnt(0)\n" \
  "s_mov_b64 s[72:73], exec\n" \
  "s_mov_b64 exec, 1\n" \
  "ds_add_u32 %[cnt], v255 offset:0\n" \
  "s_mov_b64 exec, s[72:73]\n" \
  "v_add_f32_e32 v253, v253, v83\n" \
  "v_add_f32_e32 v254, v254, v84\n" \
  "v_add_f32_e32 v253, v253, v85\n" \
  "v_add_f32_e32 v254, v254, v86\n" \
  "v_mfma_f32_32x32x16_bf16 v[184:199], v[100:103], v[52:55], v[184:199]\n" \
  "v_add_f32_e32 v253, v253, v87\n" \
  "v_mfma_f32_32x32x16_bf16 v[184:199], v[104:107], v[60:63], v[184:199]\n" \
  "v_mfma_f32_32x32x16_bf16 v[184:199], v[108:111], v[68:71], v[184:199]\n" \
  "v_mfma_f32_32x32x16_bf16 v[200:215], v[112:115], v[28:31], 0\n" \
  "s_nop 10\n" \
  "v_add_f32_e32 v254, v254, v184\n" \
  "v_mfma_f32_32x32x16_bf16 v[200:215], v[116:119], v[36:39], v[200:215]\n" \
  "v_add_f32_e32 v253, v253, v185\n" \
  "v_add_f32_e32 v254, v254, v186\n" \
  "v_add_f32_e32 v253, v253, v187\n" \
  "v_add_f32_e32 v254, v254, v188\n" \
  "v_add_f32_e32 v253, v253, v189\n" \
  "v_add_f32_e32 v254, v254, v190\n" \
  "v_mfma_f32_32x32x16_bf16 v[200:215], v[120:123], v[44:47], v[200:215]\n" \
  "v_add_f32_e32 v253, v253, v191\n" \
  "v_add_f32_e32 v254, v254, v192\n" \
  "v_add_f32_e32 v253, v253, v193\n" \
  "v_add_f32_e32 v254, v254, v194\n" \
  "v_add_f32_e32 v253, v253, v195\n" \
  "v_add_f32_e32 v254, v254, v196\n" \
  "v_mfma_f32_32x32x16_bf16 v[200:215], v[124:127], v[52:55], v[200:215]\n" \
  "v_add_f32_e32 v253, v253, v197\n" \
  "v_add_f32_e32 v254, v254, v198\n" \
  "v_add_f32_e32 v253, v253, v199\n" \
  "v_mfma_f32_32x32x16_bf16 v[200:215], v[128:131], v[60:63], v[200:215]\n" \
  "v_mfma_f32_32x32x16_bf16 v[200:215], v[132:135], v[68:71], v[200:215]\n" \
  "v_mfma_f32_32x32x16_bf16 v[216:231], v[24:27], v[136:139], 0\n" \
  "s_nop 10\n" \
  "v_add_f32_e32 v254, v254, v200\n" \
  "v_mfma_f32_32x32x16_bf16 v[216:231], v[32:35], v[140:143], v[216:231]\n" \
  "v_add_f32_e32 v253, v253, v201\n" \
  "v_add_f32_e32 v254, v254, v202\n" \
  "v_add_f32_e32 v253, v253, v203\n" \
  "v_add_f32_e32 v254, v254, v204\n" \
  "v_add_f32_e32 v253, v253, v205\n" \
  "v_add_f32_e32 v254, v254, v206\n" \
  "v_mfma_f32_32x32x16_bf16 v[216:231], v[40:43], v[144:147], v[216:231]\n" \
  "v_add_f32_e32 v253, v253, v207\n" \
  "v_add_f32_e32 v254, v254, v208\n" \
  "v_add_f32_e32 v253, v253, v209\n" \
  "v_add_f32_e32 v254, v254, v210\n" \
  "v_add_f32_e32 v253, v253, v211\n" \
  "v_add_f32_e32 v254, v254, v212\n" \
  "v_mfma_f32_32x32x16_bf16 v[216:231], v[48:51], v[148:151], v[216:231]\n" \
  "v_add_f32_e32 v253, v253, v213\n" \
  "v_add_f32_e32 v254, v254, v214\n" \
  "v_add_f32_e32 v253, v253, v215\n" \
  "v_add_f32_e32 v254, v254, v253\n" \
  "v_mov_b32_e32 v255, v254\n" \
  "v_mfma_f32_32x32x16_bf16 v[216:231], v[56:59], v[152:155], v[216:231]\n" \
  "s_nop 0\n" \
  "v_permlane32_swap_b32_e32 v254, v255\n" \
  "v_add_f32_e32 v254, v254, v255\n" \
  "v_mul_f32_e32 v254, 0x3c2aaaab, v254\n" \
  "v_sub_f32_e32 v72, v72, v254\n" \
  "v_mfma_f32_32x32x16_bf16 v[216:231], v[64:67], v[156:159], v[216:231]\n" \
  "v_sub_f32_e32 v73, v73, v254\n" \
  "v_sub_f32_e32 v74, v74, v254\n" \
  "v_sub_f32_e32 v75, v75, v254\n" \
  "v_sub_f32_e32 v76, v76, v254\n" \
  "v_sub_f32_e32 v77, v77, v254\n" \
  "v_sub_f32_e32 v78, v78, v254\n" \
  "v_sub_f32_e32 v79, v79, v254\n" \
  "v_sub_f32_e32 v80, v80, v254\n" \
  "v_sub_f32_e32 v81, v81, v254\n" \
  "v_sub_f32_e32 v82, v82, v254\n" \
  "v_sub_f32_e32 v83, v83, v254\n" \
  "v_sub_f32_e32 v84, v84, v254\n" \
  "v_sub_f32_e32 v85, v85, v254\n" \
  "v_sub_f32_e32 v86, v86, v254\n" \
  "v_sub_f32_e32 v87, v87, v254\n" \
  "v_cvt_pk_bf16_f32 v28, v72, v73\n" \
  "v_cvt_pk_bf16_f32 v29, v74, v75\n" \
  "v_cvt_pk_bf16_f32 v30, v76, v77\n" \
  "v_cvt_pk_bf16_f32 v31, v78, v79\n" \
  "ds_write_b128 %[dst], v[28:31] offset:0\n" \
  "v_cvt_pk_bf16_f32 v28, v80, v81\n" \
  "v_cvt_pk_bf16_f32 v29, v82, v83\n" \
  "v_cvt_pk_bf16_f32 v30, v84, v85\n" \
  "v_cvt_pk_bf16_f32 v31, v86, v87\n" \
  "v_mfma_f32_32x32x16_bf16 v[72:87], v[88:91], v[136:139], 0\n" \
  "ds_write_b128 %[dst], v[28:31] offset:1024\n" \
  "v_sub_f32_e32 v184, v184, v254\n" \
  "v_sub_f32_e32 v185, v185, v254\n" \
  "v_sub_f32_e32 v186, v186, v254\n" \
  "v_sub_f32_e32 v187, v187, v254\n" \
  "v_sub_f32_e32 v188, v188, v254\n" \
  "v_sub_f32_e32 v189, v189, v254\n" \
  "v_mfma_f32_32x32x16_bf16 v[72:87], v[92:95], v[140:143], v[72:87]\n" \
  "v_sub_f32_e32 v190, v190, v254\n" \
  "v_sub_f32_e32 v191, v191, v254\n" \
  "v_sub_f32_e32 v192, v192, v254\n" \
  "v_sub_f32_e32 v193, v193, v254\n" \
  "v_sub_f32_e32 v194, v194, v254\n" \
  "v_sub_f32_e32 v195, v195, v254\n" \
  "v_mfma_f32_32x32x16_bf16 v[72:87], v[96:99], v[144:147], v[72:87]\n" \
  "v_sub_f32_e32 v196, v196, v254\n" \
  "v_sub_f32_e32 v197, v197, v254\n" \
  "v_sub_f32_e32 v198, v198, v254\n" \
  "v_sub_f32_e32 v199, v199, v254\n" \
  "v_cvt_pk_bf16_f32 v28, v184, v185\n" \
  "v_cvt_pk_bf16_f32 v29, v186, v187\n" \
  "v_mfma_f32_32x32x16_bf16 v[72:87], v[100:103], v[148:151], v[72:87]\n" \
  "v_cvt_pk_bf16_f32 v30, v188, v189\n" \
  "v_cvt_pk_bf16_f32 v31, v190, v191\n" \
  "ds_write_b128 %[dst], v[28:31] offset:2048\n" \
  "v_cvt_pk_bf16_f32 v28, v192, v193\n" \
  "v_cvt_pk_bf16_f32 v29, v194, v195\n" \
  "v_mfma_f32_32x32x16_bf16 v[72:87], v[104:107], v[152:155], v[72:87]\n" \
  "v_cvt_pk_bf16_f32 v30, v196, v197\n" \
  "v_cvt_pk_bf16_f32 v31, v198, v199\n" \
  "ds_write_b128 %[dst], v[28:31] offset:3072\n" \
  "v_add_f32_e32 v255, v216, v217\n" \
  "v_add_f32_e32 v253, v218, v219\n" \
  "v_add_f32_e32 v255, v255, v220\n" \
  "v_mfma_f32_32x32x16_bf16 v[72:87], v[108:111], v[156:159], v[72:87]\n" \
  "s_waitcnt vmcnt(24)\n" \
  "s_mov_b64 s[72:73], exec\n" \
  "s_mov_b64 exec, 1\n" \
  "ds_add_u32 %[cnt2], %[flag] offset:0\n" \
  "s_mov_b64 exec, s[72:73]\n" \
  "v_add_f32_e32 v253, v253, v221\n" \
  "v_add_f32_e32 v255, v255, v222\n" \
  "v_add_f32_e32 v253, v253, v223\n" \
  "v_add_f32_e32 v255, v255, v224\n" \
  "v_add_f32_e32 v253, v253, v225\n" \
  "v_add_f32_e32 v255, v255, v226\n" \
  "v_mfma_f32_32x32x16_bf16 v[184:199], v[112:115], v[136:139], 0\n" \
  "v_sub_f32_e32 v200, v200, v254\n" \
  "v_add_f32_e32 v253, v253, v227\n" \
  "v_add_f32_e32 v255, v255, v228\n" \
  "v_add_f32_e32 v253, v253, v229\n" \
  "v_add_f32_e32 v255, v255, v230\n" \
  "v_add_f32_e32 v253, v253, v231\n" \
  "v_mfma_f32_32x32x16_bf16 v[184:199], v[116:119], v[140:143], v[184:199]\n" \
  "v_sub_f32_e32 v201, v201, v254\n" \
  "v_add_f32_e32 v255, v255, v72\n" \
  "v_add_f32_e32 v253, v253, v73\n" \
  "v_add_f32_e32 v255, v255, v74\n" \
  "v_add_f32_e32 v253, v253, v75\n" \
  "v_add_f32_e32 v255, v255, v76\n" \
  "v_mfma_f32_32x32x16_bf16 v[184:199], v[120:123], v[144:147], v[184:199]\n" \
  "v_sub_f32_e32 v202, v202, v254\n" \
  "v_add_f32_e32 v253, v253, v77\n" \
  "v_add_f32_e32 v255, v255, v78\n" \
  "v_add_f32_e32 v253, v253, v79\n" \
  "v_add_f32_e32 v255, v255, v80\n" \
  "v_add_f32_e32 v253, v253, v81\n" \
  "v_mfma_f32_32x32x16_bf16 v[184:199], v[124:127], v[148:151], v[184:199]\n" \
  "v_sub_f32_e32 v203, v203, v254\n" \
  "v_add_f32_e32 v255, v255, v82\n" \
  "v_add_f32_e32 v253, v253, v83\n" \
  "v_add_f32_e32 v255, v255, v84\n" \
  "v_add_f32_e32 v253, v253, v85\n" \
  "v_add_f32_e32 v255, v255, v86\n" \
  "v_mfma_f32_32x32x16_bf16 v[184:199], v[128:131], v[152:155], v[184:199]\n" \
  "v_sub_f32_e32 v204, v204, v254\n" \
  "v_add_f32_e32 v253, v253, v87\n" \
  "v_sub_f32_e32 v205, v205, v254\n" \
  "v_sub_f32_e32 v206, v206, v254\n" \
  "v_sub_f32_e32 v207, v207, v254\n" \
  "v_sub_f32_e32 v208, v208, v254\n" \
  "v_mfma_f32_32x32x16_bf16 v[184:199], v[132:135], v[156:159], v[184:199]\n" \
  "v_sub_f32_e32 v209, v209, v254\n" \
  "v_sub_f32_e32 v210, v210, v254\n" \
  "v_sub_f32_e32 v211, v211, v254\n" \
  "v_sub_f32_e32 v212, v212, v254\n" \
  "v_sub_f32_e32 v213, v213, v254\n" \
  "v_sub_f32_e32 v214, v214, v254\n" \
  "v_sub_f32_e32 v215, v215, v254\n" \
  "v_cvt_pk_bf16_f32 v28, v200, v201\n" \
  "v_cvt_pk_bf16_f32 v29, v202, v203\n" \
  "v_cvt_pk_bf16_f32 v30, v204, v205\n" \
  "v_cvt_pk_bf16_f32 v31, v206, v207\n" \
  "ds_write_b128 %[dst], v[28:31] offset:4096\n" \
  "v_add_f32_e32 v255, v255, v184\n" \
  "v_add_f32_e32 v253, v253, v185\n" \
  "v_add_f32_e32 v255, v255, v186\n" \
  "v_add_f32_e32 v253, v253, v187\n" \
  "v_add_f32_e32 v255, v255, v188\n" \
  "v_add_f32_e32 v253, v253, v189\n" \
  "v_add_f32_e32 v255, v255, v190\n" \
  "v_add_f32_e32 v253, v253, v191\n" \
  "v_add_f32_e32 v255, v255, v192\n" \
  "v_add_f32_e32 v253, v253, v193\n" \
  "v_add_f32_e32 v255, v255, v194\n" \
  "v_add_f32_e32 v253, v253, v195\n" \
  "v_add_f32_e32 v255, v255, v196\n" \
  "v_add_f32_e32 v253, v253, v197\n" \
  "v_add_f32_e32 v255, v255, v198\n" \
  "v_add_f32_e32 v253, v253, v199\n" \
  "v_add_f32_e32 v255, v255, v253\n" \
  "v_mov_b32_e32 v254, v255\n" \
  "v_cvt_pk_bf16_f32 v28, v208, v209\n" \
  "v_cvt_pk_bf16_f32 v29, v210, v211\n" \
  "v_cvt_pk_bf16_f32 v30, v212, v213\n" \
  "v_cvt_pk_bf16_f32 v31, v214, v215\n" \
  "v_mfma_f32_32x32x16_bf16 v[200:215], v[24:27], v[160:163], 0\n" \
  "ds_write_b128 %[dst], v[28:31] offset:5120\n" \
  "v_permlane32_swap_b32_e32 v255, v254\n" \
  "v_add_f32_e32 v255, v255, v254\n" \
  "v_mul_f32_e32 v255, 0x3c2aaaab, v255\n" \
  "v_sub_f32_e32 v216, v216, v255\n" \
  "v_mfma_f32_32x32x16_bf16 v[200:215], v[32:35], v[164:167], v[200:215]\n" \
  "v_sub_f32_e32 v217, v217, v255\n" \
  "v_sub_f32_e32 v218, v218, v255\n" \
  "v_sub_f32_e32 v219, v219, v255\n" \
  "v_sub_f32_e32 v220, v220, v255\n" \
  "v_sub_f32_e32 v221, v221, v255\n" \
  "v_sub_f32_e32 v222, v222, v255\n" \
  "v_mfma_f32_32x32x16_bf16 v[200:215], v[40:43], v[168:171], v[200:215]\n" \
  "v_sub_f32_e32 v223, v223, v255\n" \
  "v_sub_f32_e32 v224, v224, v255\n" \
  "v_sub_f32_e32 v225, v225, v255\n" \
  "v_sub_f32_e32 v226, v226, v255\n" \
  "v_sub_f32_e32 v227, v227, v255\n" \
  "v_sub_f32_e32 v228, v228, v255\n" \
  "v_mfma_f32_32x32x16_bf16 v[200:215], v[48:51], v[172:175], v[200:215]\n" \
  "v_sub_f32_e32 v229, v229, v255\n" \
  "v_sub_f32_e32 v230, v230, v255\n" \
  "v_sub_f32_e32 v231, v231, v255\n" \
  "v_cvt_pk_bf16_f32 v24, v216, v217\n" \
  "v_cvt_pk_bf16_f32 v25, v218, v219\n" \
  "v_cvt_pk_bf16_f32 v26, v220, v221\n" \
  "v_mfma_f32_32x32x16_bf16 v[200:215], v[56:59], v[176:179], v[200:215]\n" \
  "v_cvt_pk_bf16_f32 v27, v222, v223\n" \
  "ds_write_b128 %[dst], v[24:27] offset:6144\n" \
  "v_cvt_pk_bf16_f32 v24, v224, v225\n" \
  "v_cvt_pk_bf16_f32 v25, v226, v227\n" \
  "v_cvt_pk_bf16_f32 v26, v228, v229\n" \
  "v_mfma_f32_32x32x16_bf16 v[200:215], v[64:67], v[180:183], v[200:215]\n" \
  "s_waitcnt vmcnt(16)\n" \
  "s_mov_b64 s[72:73], exec\n" \
  "s_mov_b64 exec, 1\n" \
  "ds_add_u32 %[cnt2], %[flag] offset:72\n" \
  "s_mov_b64 exec, s[72:73]\n" \
  "v_cvt_pk_bf16_f32 v27, v230, v231\n" \
  "ds_write_b128 %[dst], v[24:27] offset:7168\n" \
  "v_sub_f32_e32 v72, v72, v255\n" \
  "v_sub_f32_e32 v73, v73, v255\n" \
  "v_sub_f32_e32 v74, v74, v255\n" \
  "v_sub_f32_e32 v75, v75, v255\n" \
  "v_mfma_f32_32x32x16_bf16 v[216:231], v[88:91], v[160:163], 0\n" \
  "v_sub_f32_e32 v76, v76, v255\n" \
  "v_sub_f32_e32 v77, v77, v255\n" \
  "v_sub_f32_e32 v78, v78, v255\n" \
  "v_sub_f32_e32 v79, v79, v255\n" \
  "v_sub_f32_e32 v80, v80, v255\n" \
  "v_sub_f32_e32 v81, v81, v255\n" \
  "v_mfma_f32_32x32x16_bf16 v[216:231], v[92:95], v[164:167], v[216:231]\n" \
  "v_sub_f32_e32 v82, v82, v255\n" \
  "v_sub_f32_e32 v83, v83, v255\n" \
  "v_sub_f32_e32 v84, v84, v255\n" \
  "v_sub_f32_e32 v85, v85, v255\n" \
  "v_sub_f32_e32 v86, v86, v255\n" \
  "v_sub_f32_e32 v87, v87, v255\n" \
  "v_mfma_f32_32x32x16_bf16 v[216:231], v[96:99], v[168:171], v[216:231]\n" \
  "v_cvt_pk_bf16_f32 v24, v72, v73\n" \
  "v_cvt_pk_bf16_f32 v25, v74, v75\n" \
  "v_cvt_pk_bf16_f32 v26, v76, v77\n" \
  "v_cvt_pk_bf16_f32 v27, v78, v79\n" \
  "ds_write_b128 %[dst], v[24:27] offset:8192\n" \
  "v_mfma_f32_32x32x16_bf16 v[216:231], v[100:103], v[172:175], v[216:231]\n" \
  "v_cvt_pk_bf16_f32 v24, v80, v81\n" \
  "v_cvt_pk_bf16_f32 v25, v82, v83\n" \
  "v_cvt_pk_bf16_f32 v26, v84, v85\n" \
  "v_cvt_pk_bf16_f32 v27, v86, v87\n" \
  "ds_write_b128 %[dst], v[24:27] offset:9216\n" \
  "v_mfma_f32_32x32x16_bf16 v[216:231], v[104:107], v[176:179], v[216:231]\n" \
  "v_sub_f32_e32 v184, v184, v255\n" \
  "v_add_f32_e32 v254, v200, v201\n" \
  "v_add_f32_e32 v253, v202, v203\n" \
  "v_add_f32_e32 v254, v254, v204\n" \
  "v_add_f32_e32 v253, v253, v205\n" \
  "v_add_f32_e32 v254, v254, v206\n" \
  "v_mfma_f32_32x32x16_bf16 v[216:231], v[108:111], v[180:183], v[216:231]\n" \
  "v_sub_f32_e32 v185, v185, v255\n" \
  "v_add_f32_e32 v253, v253, v207\n" \
  "v_add_f32_e32 v254, v254, v208\n" \
  "v_add_f32_e32 v253, v253, v209\n" \
  "v_add_f32_e32 v254, v254, v210\n" \
  "v_add_f32_e32 v253, v253, v211\n" \
  "v_mfma_f32_32x32x16_bf16 v[72:87], v[112:115], v[160:163], 0\n" \
  "v_sub_f32_e32 v186, v186, v255\n" \
  "v_add_f32_e32 v254, v254, v212\n" \
  "v_add_f32_e32 v253, v253, v213\n" \
  "v_add_f32_e32 v254, v254, v214\n" \
  "v_add_f32_e32 v253, v253, v215\n" \
  "v_add_f32_e32 v254, v254, v216\n" \
  "v_mfma_f32_32x32x16_bf16 v[72:87], v[116:119], v[164:167], v[72:87]\n" \
  "v_sub_f32_e32 v187, v187, v255\n" \
  "v_add_f32_e32 v253, v253, v217\n" \
  "v_add_f32_e32 v254, v254, v218\n" \
  "v_add_f32_e32 v253, v253, v219\n" \
  "v_add_f32_e32 v254, v254, v220\n" \
  "v_add_f32_e32 v253, v253, v221\n" \
  "v_mfma_f32_32x32x16_bf16 v[72:87], v[120:123], v[168:171], v[72:87]\n" \
  "v_sub_f32_e32 v188, v188, v255\n" \
  "v_add_f32_e32 v254, v254, v222\n" \
  "v_add_f32_e32 v253, v253, v223\n" \
  "v_add_f32_e32 v254, v254, v224\n" \
  "v_add_f32_e32 v253, v253, v225\n" \
  "v_add_f32_e32 v254, v254, v226\n" \
  "v_mfma_f32_32x32x16_bf16 v[72:87], v[124:127], v[172:175], v[72:87]\n" \
  "v_sub_f32_e32 v189, v189, v255\n" \
  "v_add_f32_e32 v253, v253, v227\n" \
  "v_add_f32_e32 v254, v254, v228\n" \
  "v_add_f32_e32 v253, v253, v229\n" \
  "v_add_f32_e32 v254, v254, v230\n" \
  "v_add_f32_e32 v253, v253, v231\n" \
  "v_mfma_f32_32x32x16_bf16 v[72:87], v[128:131], v[176:179], v[72:87]\n" \
  "v_sub_f32_e32 v190, v190, v255\n" \
  "v_sub_f32_e32 v191, v191, v255\n" \
  "v_sub_f32_e32 v192, v192, v255\n" \
  "v_sub_f32_e32 v193, v193, v255\n" \
  "v_sub_f32_e32 v194, v194, v255\n" \
  "v_sub_f32_e32 v195, v195, v255\n" \
  "v_mfma_f32_32x32x16_bf16 v[72:87], v[132:135], v[180:183], v[72:87]\n" \
  "s_waitcnt vmcnt(0)\n" \
  "s_mov_b64 s[72:73], exec\n" \
  "s_mov_b64 exec, 1\n" \
  "ds_add_u32 %[cnt2], %[flag] offset:8\n" \
  "s_mov_b64 exec, s[72:73]\n" \
  "v_sub_f32_e32 v196, v196, v255\n" \
  "v_sub_f32_e32 v197, v197, v255\n" \
  "v_sub_f32_e32 v198, v198, v255\n" \
  "v_sub_f32_e32 v199, v199, v255\n" \
  "v_cvt_pk_bf16_f32 v24, v184, v185\n" \
  "v_cvt_pk_bf16_f32 v25, v186, v187\n" \
  "v_cvt_pk_bf16_f32 v26, v188, v189\n" \
  "v_cvt_pk_bf16_f32 v27, v190, v191\n" \
  "ds_write_b128 %[dst], v[24:27] offset:10240\n" \
  "v_cvt_pk_bf16_f32 v24, v192, v193\n" \
  "v_cvt_pk_bf16_f32 v25, v194, v195\n" \
  "v_add_f32_e32 v254, v254, v72\n" \
  "v_add_f32_e32 v253, v253, v73\n" \
  "v_add_f32_e32 v254, v254, v74\n" \
  "v_add_f32_e32 v253, v253, v75\n" \
  "v_add_f32_e32 v254, v254, v76\n" \
  "v_add_f32_e32 v253, v253, v77\n" \
  "v_add_f32_e32 v254, v254, v78\n" \
  "v_add_f32_e32 v253, v253, v79\n" \
  "v_add_f32_e32 v254, v254, v80\n" \
  "v_add_f32_e32 v253, v253, v81\n" \
  "v_add_f32_e32 v254, v254, v82\n" \
  "v_add_f32_e32 v253, v253, v83\n" \
  "v_add_f32_e32 v254, v254, v84\n" \
  "v_add_f32_e32 v253, v253, v85\n" \
  "v_add_f32_e32 v254, v254, v86\n" \
  "v_add_f32_e32 v253, v253, v87\n" \
  "v_add_f32_e32 v254, v254, v253\n" \
  "v_mov_b32_e32 v255, v254\n" \
  "v_cvt_pk_bf16_f32 v26, v196, v197\n" \
  "v_cvt_pk_bf16_f32 v27, v198, v199\n" \
  "s_waitcnt lgkmcnt(0)\n" \
  "ds_write_b128 %[dst], v[24:27] offset:11264\n" \
  "v_permlane32_swap_b32_e32 v254, v255\n" \
  "v_add_f32_e32 v254, v254, v255\n" \
  "v_mul_f32_e32 v254, 0x3c2aaaab, v254\n" \
  "v_sub_f32_e32 v200, v200, v254\n" \
  "v_sub_f32_e32 v201, v201, v254\n" \
  "v_sub_f32_e32 v202, v202, v254\n" \
  "v_sub_f32_e32 v203, v203, v254\n" \
  "v_sub_f32_e32 v204, v204, v254\n" \
  "v_sub_f32_e32 v205, v205, v254\n" \
  "v_sub_f32_e32 v206, v206, v254\n" \
  "v_sub_f32_e32 v207, v207, v254\n" \
  "v_sub_f32_e32 v208, v208, v254\n" \
  "v_sub_f32_e32 v209, v209, v254\n" \
  "v_sub_f32_e32 v210, v210, v254\n" \
  "v_sub_f32_e32 v211, v211, v254\n" \
  "v_sub_f32_e32 v212, v212, v254\n" \
  "v_sub_f32_e32 v213, v213, v254\n" \
  "v_sub_f32_e32 v214, v214, v254\n" \
  "v_sub_f32_e32 v215, v215, v254\n" \
  "v_sub_f32_e32 v216, v216, v254\n" \
  "v_sub_f32_e32 v217, v217, v254\n" \
  "v_sub_f32_e32 v218, v218, v254\n" \
  "v_sub_f32_e32 v219, v219, v254\n" \
  "v_sub_f32_e32 v220, v220, v254\n" \
  "v_sub_f32_e32 v221, v221, v254\n" \
  "v_sub_f32_e32 v222, v222, v254\n" \
  "v_sub_f32_e32 v223, v223, v254\n" \
  "v_sub_f32_e32 v224, v224, v254\n" \
  "v_sub_f32_e32 v225, v225, v254\n" \
  "v_sub_f32_e32 v226, v226, v254\n" \
  "v_sub_f32_e32 v227, v227, v254\n" \
  "v_sub_f32_e32 v228, v228, v254\n" \
  "v_sub_f32_e32 v229, v229, v254\n" \
  "v_sub_f32_e32 v230, v230, v254\n" \
  "v_sub_f32_e32 v231, v231, v254\n" \
  "v_sub_f32_e32 v72, v72, v254\n" \
  "v_sub_f32_e32 v73, v73, v254\n" \
  "v_sub_f32_e32 v74, v74, v254\n" \
  "v_sub_f32_e32 v75, v75, v254\n" \
  "v_sub_f32_e32 v76, v76, v254\n" \
  "v_sub_f32_e32 v77, v77, v254\n" \
  "v_sub_f32_e32 v78, v78, v254\n" \
  "v_sub_f32_e32 v79, v79, v254\n" \
  "v_sub_f32_e32 v80, v80, v254\n" \
  "v_sub_f32_e32 v81, v81, v254\n" \
  "v_sub_f32_e32 v82, v82, v254\n" \
  "v_sub_f32_e32 v83, v83, v254\n" \
  "v_sub_f32_e32 v84, v84, v254\n" \
  "v_sub_f32_e32 v85, v85, v254\n" \
  "v_sub_f32_e32 v86, v86, v254\n" \
  "v_sub_f32_e32 v87, v87, v254\n" \
  "v_cvt_pk_bf16_f32 v24, v200, v201\n" \
  "v_cvt_pk_bf16_f32 v25, v202, v203\n" \
  "v_cvt_pk_bf16_f32 v26, v204, v205\n" \
  "v_cvt_pk_bf16_f32 v27, v206, v207\n" \
  "ds_write_b128 %[dst], v[24:27] offset:12288\n" \
  "v_cvt_pk_bf16_f32 v24, v208, v209\n" \
  "v_cvt_pk_bf16_f32 v25, v210, v211\n" \
  "v_cvt_pk_bf16_f32 v26, v212, v213\n" \
  "v_cvt_pk_bf16_f32 v27, v214, v215\n" \
  "ds_write_b128 %[dst], v[24:27] offset:13312\n" \
  "v_cvt_pk_bf16_f32 v24, v216, v217\n" \
  "v_cvt_pk_bf16_f32 v25, v218, v219\n" \
  "v_cvt_pk_bf16_f32 v26, v220, v221\n" \
  "v_cvt_pk_bf16_f32 v27, v222, v223\n" \
  "ds_write_b128 %[dst], v[24:27] offset:14336\n" \
  "v_cvt_pk_bf16_f32 v24, v224, v225\n" \
  "v_cvt_pk_bf16_f32 v25, v226, v227\n" \
  "v_cvt_pk_bf16_f32 v26, v228, v229\n" \
  "v_cvt_pk_bf16_f32 v27, v230, v231\n" \
  "ds_write_b128 %[dst], v[24:27] offset:15360\n" \
  "v_cvt_pk_bf16_f32 v24, v72, v73\n" \
  "v_cvt_pk_bf16_f32 v25, v74, v75\n" \
  "v_cvt_pk_bf16_f32 v26, v76, v77\n" \
  "v_cvt_pk_bf16_f32 v27, v78, v79\n" \
  "ds_write_b128 %[dst], v[24:27] offset:16384\n" \
  "v_cvt_pk_bf16_f32 v24, v80, v81\n" \
  "v_cvt_pk_bf16_f32 v25, v82, v83\n" \
  "v_cvt_pk_bf16_f32 v26, v84, v85\n" \
  "v_cvt_pk_bf16_f32 v27, v86, v87\n" \
  "ds_write_b128 %[dst], v[24:27] offset:17408\n"

#define PREP_CLOBBERS "v48", "v49", "v50", "v51", "v52", "v53", "v54", "v55", "v56", "v57", "v58", "v59", "v60", "v61", "v62", "v63", "v64", "v65", "v66", "v67", "v68", "v69", "v70", "v71", "v72", "v73", "v74", "v75", "v76", "v77", "v78", "v79", "v80", "v81", "v82", "v83", "v84", "v85", "v86", "v87", "v88", "v89", "v90", "v91", "v92", "v93", "v94", "v95", "v96", "v97", "v98", "v99", "v100", "v101", "v102", "v103", "v104", "v105", "v106", "v107", "v108", "v109", "v110", "v111", "v112", "v113", "v114", "v115", "v116", "v117", "v118", "v119", "v120", "v121", "v122", "v123", "v124", "v125", "v126", "v127", "v128", "v129", "v130", "v131", "v132", "v133", "v134", "v135", "v136", "v137", "v138", "v139", "v140", "v141", "v142", "v143", "v144", "v145", "v146", "v147", "v148", "v149", "v150", "v151", "v152", "v153", "v154", "v155", "v156", "v157", "v158", "v159", "v160", "v161", "v162", "v163", "v164", "v165", "v166", "v167", "v168", "v169", "v170", "v171", "v172", "v173", "v174", "v175", "v176", "v177", "v178", "v179", "v180", "v181", "v182", "v183", "v184", "v185", "v186", "v187", "v188", "v189", "v190", "v191", "v192", "v193", "v194", "v195", "v196", "v197", "v198", "v199", "v200", "v201", "v202", "v203", "v204", "v205", "v206", "v207", "v208", "v209", "v210", "v211", "v212", "v213", "v214", "v215", "v216", "v217", "v218", "v219", "v220", "v221", "v222", "v223", "v224", "v225", "v226", "v227", "v228", "v229", "v230", "v231", "v232", "v233", "v234", "v235", "v236", "v237", "v238", "v239", "v240", "v241", "v242", "v243", "v244", "v245", "v246", "v247", "v248", "v249", "v250", "v251", "v252", "v253", "v254", "v255", "s64", "s65", "s66", "s67", "s68", "s69", "s70", "s71", "s72", "s73", "s74", "s75", "scc", "memory"
#define T_CLOBBERS "v40", "v41", "v42", "v43", "v44", "v45", "v46", "v47", "v48", "v49", "v50", "v51", "v52", "v53", "v54", "v55", "v56", "v57", "v58", "v59", "v60", "v61", "v62", "v63", "v64", "v65", "v66", "v67", "v68", "v69", "v70", "v71", "v72", "v73", "v74", "v75", "v76", "v77", "v78", "v79", "v80", "v81", "v82", "v83", "v84", "v85", "v86", "v87", "v88", "v89", "v90", "v91", "v92", "v93", "v94", "v95", "v96", "v97", "v98", "v99", "v100", "v101", "v102", "v103", "v104", "v105", "v106", "v107", "v108", "v109", "v110", "v111", "v112", "v113", "v114", "v115", "v116", "v117", "v118", "v119", "v120", "v121", "v122", "v123", "v124", "v125", "v126", "v127", "v128", "v129", "v130", "v131", "v132", "v133", "v134", "v135", "v136", "v137", "v138", "v139", "v140", "v141", "v142", "v143", "v144", "v145", "v146", "v147", "v148", "v149", "v150", "v151", "v152", "v153", "v154", "v155", "v156", "v157", "v158", "v159", "v160", "v161", "v162", "v163", "v164", "v165", "v166", "v167", "v168", "v169", "v170", "v171", "v172", "v173", "v174", "v175", "v176", "v177", "v178", "v179", "v180", "v181", "v182", "v183", "v184", "v185", "v186", "v187", "v188", "v189", "v190", "v191", "v192", "v193", "v194", "v195", "v196", "v197", "v198", "v199", "v200", "v201", "v202", "v203", "v204", "v205", "v206", "v207", "v208", "v209", "v210", "v211", "v212", "v213", "v214", "v215", "v216", "v217", "v218", "v219", "v220", "v221", "v222", "v223", "v224", "v225", "v226", "v227", "v228", "v229", "v230", "v231", "v232", "v233", "v234", "v235", "v236", "v237", "v238", "v239", "v240", "v241", "v242", "v243", "v244", "v245", "v246", "v247", "v248", "v249", "v250", "v251", "v252", "v253", "v254", "v255", "s64", "s65", "s66", "s67", "s68", "s69", "s70", "s71", "s72", "s73", "s74", "s75", "scc", "memory"
#define BLOCK_CLOBBERS "v24", "v25", "v26", "v27", "v28", "v29", "v30", "v31", "v32", "v33", "v34", "v35", "v36", "v37", "v38", "v39", "v40", "v41", "v42", "v43", "v44", "v45", "v46", "v47", "v48", "v49", "v50", "v51", "v52", "v53", "v54", "v55", "v56", "v57", "v58", "v59", "v60", "v61", "v62", "v63", "v64", "v65", "v66", "v67", "v68", "v69", "v70", "v71", "v72", "v73", "v74", "v75", "v76", "v77", "v78", "v79", "v80", "v81", "v82", "v83", "v84", "v85", "v86", "v87", "v88", "v89", "v90", "v91", "v92", "v93", "v94", "v95", "v96", "v97", "v98", "v99", "v100", "v101", "v102", "v103", "v104", "v105", "v106", "v107", "v108", "v109", "v110", "v111", "v112", "v113", "v114", "v115", "v116", "v117", "v118", "v119", "v120", "v121", "v122", "v123", "v124", "v125", "v126", "v127", "v128", "v129", "v130", "v131", "v132", "v133", "v134", "v135", "v136", "v137", "v138", "v139", "v140", "v141", "v142", "v143", "v144", "v145", "v146", "v147", "v148", "v149", "v150", "v151", "v152", "v153", "v154", "v155", "v156", "v157", "v158", "v159", "v160", "v161", "v162", "v163", "v164", "v165", "v166", "v167", "v168", "v169", "v170", "v171", "v172", "v173", "v174", "v175", "v176", "v177", "v178", "v179", "v180", "v181", "v182", "v183", "v184", "v185", "v186", "v187", "v188", "v189", "v190", "v191", "v192", "v193", "v194", "v195", "v196", "v197", "v198", "v199", "v200", "v201", "v202", "v203", "v204", "v205", "v206", "v207", "v208", "v209", "v210", "v211", "v212", "v213", "v214", "v215", "v216", "v217", "v218", "v219", "v220", "v221", "v222", "v223", "v224", "v225", "v226", "v227", "v228", "v229", "v230", "v231", "v232", "v233", "v234", "v235", "v236", "v237", "v238", "v239", "v240", "v241", "v242", "v243", "v244", "v245", "v246", "v247", "v248", "v249", "v250", "v251", "v252", "v253", "v254", "v255", "s64", "s65", "s66", "s67", "s68", "s69", "s70", "s71", "s72", "s73", "s74", "s75", "scc", "memory"

#define DMA_HEAD "s_mov_b32 %0, m0\n\ts_mov_b32 m0, %[m]\n\ts_nop 0\n\t"
#define DMA_TAIL "s_mov_b32 m0, %0"
DEV void dma3(const void* sb, unsigned m, unsigned v0, unsigned v1, unsigned v2) {
  unsigned keep; m = __builtin_amdgcn_readfirstlane(m); sb = uptr(sb);
  asm volatile("s_nop 1\n\t" DMA_HEAD "global_load_lds_dwordx4 %[a], %[s]\n\tglobal_load_lds_dwordx4 %[b], %[s] offset:1024\n\t"
               "global_load_lds_dwordx4 %[c], %[s] offset:2048\n\t" DMA_TAIL
               : "=&s"(keep) : [m] "s"(m), [s] "s"(sb), [a] "v"(v0), [b] "v"(v1), [c] "v"(v2) : "memory");
}
DEV void dma4(const void* sb, unsigned m, unsigned v0, unsigned v1, unsigned v2, unsigned v3) {
  unsigned keep; m = __builtin_amdgcn_readfirstlane(m); sb = uptr(sb);
  asm volatile("s_nop 1\n\t" DMA_HEAD "global_load_lds_dwordx4 %[a], %[s]\n\tglobal_load_lds_dwordx4 %[b], %[s] offset:1024\n\t"
               "global_load_lds_dwordx4 %[c], %[s] offset:2048\n\tglobal_load_lds_dwordx4 %[d], %[s] offset:3072\n\t" DMA_TAIL
               : "=&s"(keep) : [m] "s"(m), [s] "s"(sb), [a] "v"(v0), [b] "v"(v1), [c] "v"(v2), [d] "v"(v3) : "memory");
}
template <int O0, int O1, bool LAST> DEV void dma2(const void* sb, unsigned m, unsigned v0, unsigned v1) {
  unsigned keep; unsigned long long ex; m = __builtin_amdgcn_readfirstlane(m); sb = uptr(sb);
  if (LAST)
    asm volatile("s_nop 1\n\t" DMA_HEAD "global_load_lds_dwordx4 %[a], %[s] offset:%c[o0]\n\ts_mov_b64 %1, exec\n\ts_mov_b64 exec, 0xfff\n\t"
                 "global_load_lds_dwordx4 %[b], %[s] offset:%c[o1]\n\ts_mov_b64 exec, %1\n\t" DMA_TAIL
                 : "=&s"(keep), "=&s"(ex) : [m] "s"(m), [s] "s"(sb), [a] "v"(v0), [b] "v"(v1), [o0] "i"(O0), [o1] "i"(O1) : "memory");
  else
    asm volatile("s_nop 1\n\t" DMA_HEAD "global_load_lds_dwordx4 %[a], %[s] offset:%c[o0]\n\tglobal_load_lds_dwordx4 %[b], %[s] offset:%c[o1]\n\t" DMA_TAIL
                 : "=&s"(keep) : [m] "s"(m), [s] "s"(sb), [a] "v"(v0), [b] "v"(v1), [o0] "i"(O0), [o1] "i"(O1) : "memory");
}
template <int NPER> DEV void stage_qk(const char* wqkv, unsigned lds_dst, int per0, int lane) {
  unsigned rowp[3], cp[3];
#pragma unroll
  for (int m = 0; m < 3; ++m) {
    const int u = 64 * m + lane, row = u / 24, f = u - 24 * row;
    rowp[m] = row * 576 + 3072 - 1024 * m;
    cp[m] = 16 * ((f & 24) | ((f & 7) ^ (row >> 1)));
  }
#pragma unroll
  for (int k = 0; k < NPER; ++k) {
    const int per = per0 + k, x = (per & 1) * 64;
    dma3(wqkv + per * (8 * 576) - 3072, lds_dst + per * 3072, rowp[0] + (cp[0] ^ x), rowp[1] + (cp[1] ^ x), rowp[2] + (cp[2] ^ x));
  }
}
template <int NPER> DEV void stage_v(const char* wqkv, unsigned lds_dst, int per0, int lane) {
  unsigned off[3];
#pragma unroll
  for (int m = 0; m < 3; ++m) {
    const int u = 64 * m + lane, row = u / 12, f = u - 12 * row;
    off[m] = row * 576 + 384 + 16 * (f ^ ((row >> 2) & 3)) + 3072 - 1024 * m;
  }
#pragma unroll
  for (int k = 0; k < NPER; ++k) dma3(wqkv + (per0 + k) * (16 * 576) - 3072, lds_dst + (per0 + k) * 3072, off[0], off[1], off[2]);
}
template <int NPER> DEV void stage_o(const char* wo, unsigned lds_dst, int per0, int lane) {
#pragma unroll
  for (int k = 0; k < NPER; ++k) dma3(wo + (per0 + k) * 3072, lds_dst + (per0 + k) * 3072, lane * 16, lane * 16, lane * 16);
}
DEV void frame_offsets(unsigned (&vo)[10], int t, int lane) {
#pragma unroll
  for (int m = 0; m < 3; ++m) {
    const int u = 64 * m + lane, P0 = u / 12, f = u - 12 * P0;
    const unsigned c16 = (unsigned)((f ^ (((P0 >> 2) + 2 * t) & 3)) << 4);
#pragma unroll
    for (int k = 0; k < 4; ++k) {
      const int i = m + 3 * k;
      if (i < 10) {
        const int P = P0 + 16 * k;
        vo[i] = (unsigned)((P * 37) >> 8) * (unsigned)(ROW_BYTES - 7 * 192) + (unsigned)P * 192u + c16 + 3072 - 1024 * (i & 3);
      }
    }
  }
  vo[9] = lane < 12 ? vo[9] : 0x80000000u;
}
DEV const char* win_base(const char* x, int col, int t) {
  return x + ((size_t)t * FRAME_BYTES + (size_t)((col >> 5) * 7) * ROW_BYTES + (col & 31) * (7 * 192)) - 3072;
}
DEV void dma_early(const char* xs, unsigned xd, const unsigned (&vo)[10]) {
  dma2<2048, 3072, false>(xs, xd + 4096, vo[6], vo[7]);
  dma2<0, 1024, true>(xs, xd + 8192, vo[8], vo[9]);
}
DEV void dma_mid4(const char* x, int col, unsigned xd, const unsigned (&vo)[10], const unsigned (&vp)[10]) {
#pragma unroll
  for (int f = 0; f < 4; ++f) {
    if (f & 1) dma2<0, 1024, false>(win_base(x, col, f), xd + f * 9408 + 4096, vp[4], vp[5]);
    else dma2<0, 1024, false>(win_base(x, col, f), xd + f * 9408 + 4096, vo[4], vo[5]);
  }
}
DEV void dma_late4(const char* x, int col, unsigned xd, const unsigned (&vo)[10], const unsigned (&vp)[10]) {
#pragma unroll
  for (int f = 0; f < 4; ++f) {
    if (f & 1) dma4(win_base(x, col, f), xd + f * 9408, vp[0], vp[1], vp[2], vp[3]);
    else dma4(win_base(x, col, f), xd + f * 9408, vo[0], vo[1], vo[2], vo[3]);
  }
}
DEV void run_pa(unsigned raw, unsigned dstw, unsigned rho, unsigned cntaddr, int jt, int lane) {
  const int r = lane & 31, h = lane >> 5, sr = sigma32(r), x = (sr >> 1) & 7;
  unsigned bs[3][4], dst[3], rr[3];
#pragma unroll
  for (int d = 0; d < 3; ++d) {
    int it = jt + d; it = it >= 3 ? it - 3 : it;
#pragma unroll
    for (int j = 0; j < 4; ++j) bs[d][j] = raw + (32 * it + sr) * 384 + 16 * ((2 * j | h) ^ x);
    dst[d] = dstw + (jt * 6 + 2 * it) * 1024 + lane * 16;
    rr[d] = rho + (32 * it + 8 * h) * 4;
  }
  const unsigned rhow = rho + (32 * jt + sr) * 4;
  asm volatile(PA_BLOCK_ASM ::
    [b00] "v"(bs[0][0]), [b01] "v"(bs[0][1]), [b02] "v"(bs[0][2]), [b03] "v"(bs[0][3]),
    [b10] "v"(bs[1][0]), [b11] "v"(bs[1][1]), [b12] "v"(bs[1][2]), [b13] "v"(bs[1][3]),
    [b20] "v"(bs[2][0]), [b21] "v"(bs[2][1]), [b22] "v"(bs[2][2]), [b23] "v"(bs[2][3]),
    [cnt] "v"(cntaddr), [rhow] "v"(rhow), [dst0] "v"(dst[0]), [dst1] "v"(dst[1]), [dst2] "v"(dst[2]),
    [rhor0] "v"(rr[0]), [rhor1] "v"(rr[1]), [rhor2] "v"(rr[2]), [tgt2] "i"(1) : PREP_CLOBBERS);
}
DEV void run_pb(unsigned vraw, unsigned oraw, unsigned cntaddr, unsigned cnt2, unsigned flag, int lane) {
  const int r = lane & 31, h = lane >> 5, sr = sigma32(r), a = (sr >> 2) & 3;
  const unsigned ve = vraw + sr * 192 + 16 * (h ^ a), vo = vraw + sr * 192 + 16 * ((2 + h) ^ a);
  const int blk = (lane >> 4) & 1, q = (lane & 15) >> 2, p = lane & 3, psw = ((p & 1) << 1) | (p >> 1);
  const unsigned tb = oraw + (8 * h + q) * 192 + (16 * blk + 4 * psw) * 2, dst = oraw + lane * 16;
  asm volatile(PB_BLOCK_ASM :: [ve] "v"(ve), [vo] "v"(vo), [tb] "v"(tb), [dst] "v"(dst), [cnt] "v"(cntaddr), [cnt2] "v"(cnt2), [flag] "v"(flag)
               : BLOCK_CLOBBERS);
}

__global__ NOPK __launch_bounds__(512, 2) void swin_main(const char* __restrict__ x, char* __restrict__ out, const char* __restrict__ wqkv_t,
                                                         const char* __restrict__ wo_t, const char* __restrict__ wqkv_s, const char* __restrict__ wo_s) {
  extern __shared__ __attribute__((aligned(16))) char smem_[];
  LAS char* smem = (LAS char*)smem_;
  const int lane = __builtin_amdgcn_workitem_id_x() & 63;
  const int w = __builtin_amdgcn_readfirstlane(__builtin_amdgcn_workitem_id_x() >> 6);
  const int g = w >> 2, t = w & 3;
  const unsigned lds0 = __builtin_amdgcn_readfirstlane((unsigned)(size_t)smem);
  {
    unsigned long long pc; unsigned len, len2;
    asm volatile("s_getpc_b64 %0\n.Lswin_code_begin:\n\ts_mov_b32 %1, .Lswin_code_end-.Lswin_code_begin\n\ts_mov_b32 %2, .Lswin_code_end2-.Lswin_code_begin"
                 : "=s"(pc), "=s"(len), "=s"(len2));
    len = len > len2 ? len : len2;
    unsigned off = (unsigned)__builtin_amdgcn_workitem_id_x() * 128u;
    off = off < len - 4u ? off : len - 4u;
    unsigned keep;
    asm volatile("s_mov_b32 %0, m0\n\ts_mov_b32 m0, %3\n\ts_nop 0\n\tglobal_load_lds_dword %1, %2\n\ts_mov_b32 m0, %0"
                 : "=&s"(keep) : "v"(off), "s"(pc), "s"(lds0 + LDS_DUMP) : "memory");
  }
  const __amdgpu_buffer_rsrc_t orsrc = __builtin_amdgcn_make_buffer_rsrc(out - 3072, 0, 4 * FRAME_BYTES + 3072, 0x00020000);
  LAS float* r2 = (LAS float*)(smem + LDS_R + g * 1024);
  LAS unsigned* cnt = (LAS unsigned*)(smem + LDS_CNT);
  const int bid = __builtin_amdgcn_workgroup_id_x();
  const unsigned xdst = lds0 + LDS_X + g * XBUF_BYTES + t * (49 * 192);
  __builtin_amdgcn_s_setprio(3);
  unsigned h0e, h0o, h1e, h1o, ws, rka, rq0a, rq1a, lin, tabv;
  const unsigned xb0 = lds0 + LDS_X + g * XBUF_BYTES, r20 = lds0 + LDS_R + g * 1024;
  auto lane_addresses = [&]() NOPK {
    const int l = opaque(lane), r = l & 31, h = l >> 5;
    const int j1 = imin(32 + r, 48);
    const int a0 = ((r >> 2) + 2 * t) & 3, a1 = r <= 16 ? a0 : 0;
    const unsigned row0 = xb0 + 192 * (49 * t + r);
    const unsigned row1 = r <= 16 ? row0 : lds0 + LDS_PADW;
    h0e = row0 + 16 * (h ^ a0); h0o = row0 + 16 * ((2 + h) ^ a0);
    h1e = row1 + 16 * (h ^ a1); h1o = row1 + 16 * ((2 + h) ^ a1);
    ws = lds0 + LDS_W + 2 * WMAT_BYTES + l * 16;
    rka = r20 + t * 256 + 16 * h; rq0a = r20 + t * 256 + 4 * r; rq1a = r20 + t * 256 + 4 * j1;
    lin = xdst + l * 16;
    tabv = lds0 + LDS_TAB + (t & 1) * 2560 + l * 4;
    asm volatile("" : "+v"(h0e), "+v"(h0o), "+v"(h1e), "+v"(h1o), "+v"(ws), "+v"(rka), "+v"(rq0a), "+v"(rq1a), "+v"(lin), "+v"(tabv));
  };
  asm volatile("" :: "s"(x), "s"(out), "s"(wqkv_t), "s"(wo_t), "s"(wqkv_s), "s"(wo_s));

  {
    const unsigned w0 = lds0 + LDS_W, c0 = lds0 + LDS_CNT, xa = lds0 + LDS_X, xb = lds0 + LDS_X + XBUF_BYTES;
    auto lds_init = [&]() NOPK {
      const int l = opaque(lane);
      if (l < 16) r2[t * 64 + 48 + l] = 0.f;
      if (w == 0 && l < 48) *(LAS unsigned*)(smem + LDS_PADR + l * 4) = 0u;
      if (w == 0 && l < 32) cnt[l] = 0u;
    };
    if (__builtin_expect(g == 0, 1)) {
      stage_qk<3>(wqkv_t, w0 + 2 * WMAT_BYTES, 3 * t, lane);
      if (t < 2) stage_v<3>(wqkv_t, w0, 3 * t, lane);
      else stage_o<3>(wo_t, w0 + WMAT_BYTES, 3 * (t - 2), lane);
      lds_init();
      wg_barrier();
      lane_addresses();
      VM_WAIT(0);
      lds_arrive(cnt + C_TR);
      lds_wait_ge(cnt + C_TR, 4);
    } else {
      unsigned vo[10], vp[10];
      if (t < 2) {
        frame_offsets(vo, 0, lane);
        frame_offsets(vp, 1, lane);
      }
      lane_addresses();
      lds_init();
      wg_barrier();
      if (t < 2) {
        dma_early(win_base(x, bid * 4, 2 * t), xa + (2 * t) * 9408, vo);
        dma_early(win_base(x, bid * 4, 2 * t + 1), xa + (2 * t + 1) * 9408, vp);
      }
      if (t == 0) {
        LAS unsigned* tab = (LAS unsigned*)(smem + LDS_TAB);
#pragma unroll
        for (int i = 0; i < 10; ++i) { tab[i * 64 + lane] = vo[i]; tab[640 + i * 64 + lane] = vp[i]; }
        VM_WAIT(0);
        lds_add(cnt + C_E, 2);
      } else if (t == 1) {
        dma_mid4(x, bid * 4, xa, vo, vp);
        VM_WAIT(8);
        lds_add(cnt + C_E, 2);
        dma_late4(x, bid * 4, xa, vo, vp);
        VM_WAIT(16);
        lds_add(cnt + C_M, 4);
        VM_WAIT(0);
        lds_add(cnt + C_L, 4);
      } else {
        stage_qk<6>(wqkv_s, xb, 6 * (t - 2), lane);
        VM_WAIT(0);
        lds_arrive(cnt + C_QS);
      }
      if (t != 0) lds_wait_ge(cnt + C_QS, 2);
    }
    if (w == 4) {
      lds_wait_ge(cnt + C_QTC, 3);
      stage_v<6>(wqkv_s, w0 + 2 * WMAT_BYTES, 0, lane);
      stage_o<6>(wo_s, w0 + 3 * WMAT_BYTES, 0, lane);
      lds_wait_ge(cnt + C_QSC, 3);
      unsigned vo[10], vp[10];
      LAS const unsigned* tab = (LAS const unsigned*)(smem + LDS_TAB);
      const int lq = opaque(lane);
#pragma unroll
      for (int i = 0; i < 10; ++i) { vo[i] = tab[i * 64 + lq]; vp[i] = tab[640 + i * 64 + lq]; }
#pragma unroll
      for (int f = 0; f < 4; ++f) {
        if (f & 1) dma_early(win_base(x, bid * 4 + 1, f), xb + f * 9408, vp);
        else dma_early(win_base(x, bid * 4 + 1, f), xb + f * 9408, vo);
      }
      dma_mid4(x, bid * 4 + 1, xb, vo, vp);
      dma_late4(x, bid * 4 + 1, xb, vo, vp);
      VM_WAIT(40);
    }
    const int role = g ? (t == 0 ? 3 : t - 1) : t;
    const int lp = opaque(lane);
    if (role < 3) run_pa(g ? xb : w0 + 2 * WMAT_BYTES, w0 + 2 * g * WMAT_BYTES, lds0 + LDS_RHO + 384 * g, c0 + 4 * (g ? C_QSC : C_QTC), role, lp);
    else run_pb(w0 + 2 * g * WMAT_BYTES, w0 + (2 * g + 1) * WMAT_BYTES, c0 + 4 * (g ? C_VSC : C_VTC), c0 + 4 * g, g ? 4u : 0u, lp);
    lds_arrive(cnt + C_WT + g);
  }

  const int lm = opaque(lane);
  const unsigned wt = ws - 2 * WMAT_BYTES;
#pragma unroll 1
  for (int round = 0; round < 2; ++round) {
    const int col = bid * 4 + round * 2 + g;
    if (round != 0) {
      VM_WAIT(14);
      lds_arrive(cnt + C_M + g);
    }
    __builtin_amdgcn_s_setprio(2);
    const int rho = (t + 3 * g + 2 * round) & 3;
    lds_wait_ge(cnt + (rho == 3 ? C_M : C_E) + g, 4 * round + 4);
    if (round == 0) lds_wait_ge(cnt + C_WT, 4);
    unsigned tb_e, tb_o, tb_r;
    {
      const int ln = opaque(lm), tf = ln & 3, hh = ln >> 5, P = rho * 8 + ((ln & 31) >> 2);
      const int a = ((P >> 2) + 2 * tf) & 3;
      const unsigned rowb = xb0 + 192 * (49 * tf + P);
      tb_e = rowb + 16 * (hh ^ a); tb_o = rowb + 16 * ((2 + hh) ^ a); tb_r = r20 + (tf * 64 + P) * 4;
    }
#pragma unroll 1
    for (int tt = 0; tt < 2; ++tt) {
      if (tt == 1) {
        if (rho == 3) break;
        lds_wait_ge(cnt + C_L + g, 4 * round + 4);
        if (rho == 2) lds_wait_ge(cnt + C_M + g, 4 * round + 4);
      }
      const unsigned up = (tt == 0 && rho != 3) ? 1u : 0u;
      unsigned ae = tb_e + up * 6144u, ao = tb_o + up * 6144u, r2a = tb_r + up * 128u;
      if (up && rho == 2) {
        const bool pad = (lm & 31) >= 4;
        const unsigned hh16 = (lm >> 5) * 16;
        ae = pad ? lds0 + LDS_PADR + hh16 : ae; ao = pad ? lds0 + LDS_PADR + 32 + hh16 : ao; r2a = pad ? lds0 + LDS_PADW : r2a;
      }
      const unsigned cl = lds0 + LDS_CNT + 4 * (C_L + g), fl = (tt == 0 && round != 0) ? 1u : 0u;
      asm volatile(T_BLOCK_ASM :: [ae] "v"(ae), [ao] "v"(ao), [wt] "v"(wt), [r2a] "v"(r2a), [cnt] "v"(cl), [flag] "v"(fl) : T_CLOBBERS);
      asm volatile(".Lswin_code_end2:");
    }
    lds_arrive(cnt + C_B3 + g);
    lds_wait_ge(cnt + C_B3 + g, 4 * round + 4);
    if (round == 0) lds_wait_ge(cnt + C_WS, 4);
    __builtin_amdgcn_s_setprio(0);
    {
      const unsigned ocol = __builtin_amdgcn_readfirstlane(t * FRAME_BYTES + ((col >> 5) * 7) * ROW_BYTES + (col & 31) * (7 * 192));
      const unsigned round_s = __builtin_amdgcn_readfirstlane(round), xdst_s = __builtin_amdgcn_readfirstlane(xdst);
      const char* xsrc = uptr(win_base(x, col + 2, t));
      asm volatile(S_BLOCK_ASM ::
        [h0e] "v"(h0e), [h0o] "v"(h0o), [h1e] "v"(h1e), [h1o] "v"(h1o), [ws] "v"(ws), [rka] "v"(rka), [rq0a] "v"(rq0a), [rq1a] "v"(rq1a),
        [lin] "v"(lin), [tab] "v"(tabv), [cnt] "v"(lds0 + LDS_CNT + 4 * (C_E + g)), [flag] "v"(round == 0 ? 1u : 0u),
        [orsrc] "s"(orsrc), [ocol] "s"(ocol), [xsrc] "s"(xsrc), [xdst] "s"(xdst_s), [round] "s"(round_s)
        : BLOCK_CLOBBERS);
      asm volatile(".Lswin_code_end:");
    }
  }
}

extern "C" void kernel_launch(void* const* d_in, const int* in_sizes, int n_in, void* d_out, int out_size, void* d_ws,
                              size_t ws_size, hipStream_t stream) {
  (void)in_sizes; (void)n_in; (void)out_size; (void)ws_size; (void)d_ws;
  (void)hipFuncSetAttribute((const void*)swin_main, hipFuncAttributeMaxDynamicSharedMemorySize, LDS_BYTES);
  swin_main<<<256, 512, LDS_BYTES, stream>>>((const char*)d_in[0], (char*)d_out, (const char*)d_in[1], (const char*)d_in[2],
                                             (const char*)d_in[3], (const char*)d_in[4]);
}
```
